```python
import torch

def _softmax(x, axis=-1):
    return torch.softmax(x, dim=axis)
def _gelu(x):
    return torch.nn.functional.gelu(x, approximate="tanh")
def _silu(x):
    return torch.nn.functional.silu(x)
def _sigmoid(x):
    return torch.sigmoid(x)
def _layernorm(x, axis=-1, eps=1e-5):
    m = x.mean(dim=axis, keepdim=True)
    v = x.var(dim=axis, keepdim=True, unbiased=False)
    return (x - m) / torch.sqrt(v + eps)
def _scatter_add(tgt, idx, src, axis=0):
    return tgt.clone().index_add_(axis, idx, src)
def _segment_sum(src, seg, n):
    out = torch.zeros((n,) + src.shape[1:], dtype=src.dtype)
    return out.index_add_(0, seg, src)
def _pad(x, pw):
    flat = tuple(v for pair in reversed(pw) for v in pair)
    return torch.nn.functional.pad(x, flat)

kernel_name = 'sequential_preLN__compressed_kv_cla__swiglu_ffn__decode'

def setup_inputs():

    _w = (lambda a: (lambda t: t.to(torch.bfloat16) if t.is_floating_point() else t)(torch.as_tensor(a)))
    def _A_setup():
        B, H, Sq, Sk, D, M = 2, 32, 1, 16384, 128, 576
        Dc = 4 * D
        return {
            "x": _w((lambda *s: torch.randn(*s, dtype=torch.bfloat16) * 0.05)(B, Sq, M)),
            "c_kv": _w((lambda *s: torch.randn(*s, dtype=torch.bfloat16) * 0.4)(B, Sk, Dc)),
            "w_q": _w((lambda *s: torch.randn(*s, dtype=torch.bfloat16) * 3.2)(M, H, D) * (M ** -0.5)),
            "w_uk": _w((lambda *s: torch.randn(*s, dtype=torch.bfloat16) * 3.2)(Dc, H, D) * (Dc ** -0.5)),
            "w_uv": _w((lambda *s: torch.randn(*s, dtype=torch.bfloat16) * 0.4)(Dc, H, D) * (Dc ** -0.5)),
            "w_o": _w((lambda *s: torch.randn(*s, dtype=torch.bfloat16) * 0.4)(H, D, M) * ((H * D) ** -0.5)),
        }
    _A_in = _A_setup()

    def _B_setup():
        B, S, D, F = 2, 1, 576, 4608
        return {
            "x": _w((lambda *s: torch.randn(*s, dtype=torch.bfloat16) * 0.05)(B, S, D)),
            "w_gate": _w((lambda *s: torch.randn(*s, dtype=torch.bfloat16) * 0.05)(D, F)),
            "w_up": _w((lambda *s: torch.randn(*s, dtype=torch.bfloat16) * 0.05)(D, F)),
            "w_down": _w((lambda *s: torch.randn(*s, dtype=torch.bfloat16) * 0.05)(F, D)),
        }
    _B_in = _B_setup()

    return {
        "A_x": _A_in["x"],
        "A_c_kv": _A_in["c_kv"],
        "A_w_q": _A_in["w_q"],
        "A_w_uk": _A_in["w_uk"],
        "A_w_uv": _A_in["w_uv"],
        "A_w_o": _A_in["w_o"],
        "B_x": _B_in["x"],
        "B_w_gate": _B_in["w_gate"],
        "B_w_up": _B_in["w_up"],
        "B_w_down": _B_in["w_down"],
    }

def reference(A_x, A_c_kv, A_w_q, A_w_uk, A_w_uv, A_w_o, B_x, B_w_gate, B_w_up, B_w_down):
    def _A_ref(x, c_kv, w_q, w_uk, w_uv, w_o):
        B, Sq, M = x.shape[0], x.shape[1], x.shape[2]
        Sk = c_kv.shape[1]
        H, D = w_q.shape[1], w_q.shape[2]
        q = torch.einsum("bqm,mhd->bhqd", x, w_q)
        k = torch.einsum("bkc,chd->bhkd", c_kv, w_uk)
        v = torch.einsum("bkc,chd->bhkd", c_kv, w_uv)
        scores = torch.einsum("bhqd,bhkd->bhqk", q, k) * (D ** -0.5)
        off = Sk - Sq
        qi = torch.arange(Sq)[:, None]
        kj = torch.arange(Sk)[None, :]
        cmask = kj <= (qi + off)
        scores = torch.where(cmask[None, None], scores, -1e9)
        p = _softmax(scores, axis=-1)
        ctx = torch.einsum("bhqk,bhkd->bhqd", p, v)
        return torch.einsum("bhqd,hdm->bqm", ctx, w_o)

    def _B_ref(x, w_gate, w_up, w_down):
        g = _silu(x @ w_gate)
        u = x @ w_up
        return (g * u) @ w_down

    x = A_x
    h = x + _A_ref(_layernorm(x, axis=-1), A_c_kv, A_w_q, A_w_uk, A_w_uv, A_w_o)
    h2 = h + _B_ref(_layernorm(h, axis=-1), B_w_gate, B_w_up, B_w_down)
    return h2
```

```cpp
#include <hip/hip_runtime.h>
#include <hip/hip_bf16.h>
#include <stdint.h>

constexpr int B=2,H=32,SK=16384,D=128,M=576,DC=512,F=4608;
typedef unsigned short u16;
__device__ __forceinline__ float bf2f(u16 v){ return __uint_as_float(((unsigned)v)<<16); }
__device__ __forceinline__ u16 f2bf(float f){ __hip_bfloat16 b = __float2bfloat16(f); return *reinterpret_cast<u16*>(&b); }

__device__ float block_sum(float v, float* red){
  int t=threadIdx.x; red[t]=v; __syncthreads();
  for(int s=blockDim.x/2;s>0;s>>=1){ if(t<s) red[t]+=red[t+s]; __syncthreads(); }
  float r=red[0]; __syncthreads(); return r;
}
__global__ void k_ln_x(const u16* x, float* xn){
  __shared__ float red[256];
  int b=blockIdx.x; float s=0; for(int i=threadIdx.x;i<M;i+=256) s+=bf2f(x[b*M+i]);
  float mean=block_sum(s,red)/M; float v=0; for(int i=threadIdx.x;i<M;i+=256){ float d=bf2f(x[b*M+i])-mean; v+=d*d; }
  float var=block_sum(v,red)/M; float r=1.0f/sqrtf(var+1e-5f);
  for(int i=threadIdx.x;i<M;i+=256) xn[b*M+i]=(bf2f(x[b*M+i])-mean)*r;
}
__global__ void k_ln_f(const float* x, float* xn){
  __shared__ float red[256];
  int b=blockIdx.x; float s=0; for(int i=threadIdx.x;i<M;i+=256) s+=x[b*M+i];
  float mean=block_sum(s,red)/M; float v=0; for(int i=threadIdx.x;i<M;i+=256){ float d=x[b*M+i]-mean; v+=d*d; }
  float var=block_sum(v,red)/M; float r=1.0f/sqrtf(var+1e-5f);
  for(int i=threadIdx.x;i<M;i+=256) xn[b*M+i]=(x[b*M+i]-mean)*r;
}
__global__ void k_gemv(const float* x, const u16* W, float* y, int K, int N){
  int n=blockIdx.x*blockDim.x+threadIdx.x; if(n>=N) return;
  float a0=0,a1=0; for(int k=0;k<K;k++){ float w=bf2f(W[(size_t)k*N+n]); a0+=x[k]*w; a1+=x[K+k]*w; }
  y[n]=a0; y[N+n]=a1;
}
__global__ void k_qabs(const float* q, const u16* wuk, float* qa){
  int i=blockIdx.x*blockDim.x+threadIdx.x; int c=i%DC, h=(i/DC)%H, b=i/(DC*H);
  float a=0; for(int d=0;d<D;d++) a+=q[(b*H+h)*D+d]*bf2f(wuk[((size_t)c*H+h)*D+d]);
  qa[i]=a;
}
__global__ void k_scores(const float* qa, const u16* ckv, float* sc){
  __shared__ float q[DC];
  int bh=blockIdx.y; int b=bh/H; for(int i=threadIdx.x;i<DC;i+=blockDim.x) q[i]=qa[bh*DC+i]; __syncthreads();
  int k=blockIdx.x*blockDim.x+threadIdx.x; const u16* r=ckv+((size_t)b*SK+k)*DC; float a=0;
  for(int c=0;c<DC;c++) a+=q[c]*bf2f(r[c]);
  sc[(size_t)bh*SK+k]=a*0.08838834764831845f;
}
__global__ void k_softmax(float* sc){
  __shared__ float red[256];
  float* s=sc+(size_t)blockIdx.x*SK; float m=-1e30f; for(int i=threadIdx.x;i<SK;i+=256) m=fmaxf(m,s[i]);
  int t=threadIdx.x; red[t]=m; __syncthreads(); for(int st=128;st>0;st>>=1){ if(t<st) red[t]=fmaxf(red[t],red[t+st]); __syncthreads(); }
  m=red[0]; __syncthreads(); float l=0; for(int i=threadIdx.x;i<SK;i+=256){ float e=expf(s[i]-m); s[i]=e; l+=e; }
  l=block_sum(l,red); for(int i=threadIdx.x;i<SK;i+=256) s[i]/=l;
}
__global__ void k_pv(const float* p, const u16* ckv, float* cc){
  int bh=blockIdx.x; int b=bh/H; int c=threadIdx.x; float a=0;
  for(int k=0;k<SK;k++) a+=p[(size_t)bh*SK+k]*bf2f(ckv[((size_t)b*SK+k)*DC+c]);
  cc[bh*DC+c]=a;
}
__global__ void k_uv(const float* cc, const u16* wuv, float* ctx){
  int i=blockIdx.x*blockDim.x+threadIdx.x; int d=i%D, h=(i/D)%H, b=i/(D*H);
  float a=0; for(int c=0;c<DC;c++) a+=cc[(b*H+h)*DC+c]*bf2f(wuv[((size_t)c*H+h)*D+d]);
  ctx[i]=a;
}
__global__ void k_addx(const u16* x, const float* o, float* h){ int i=blockIdx.x*blockDim.x+threadIdx.x; if(i<B*M) h[i]=bf2f(x[i])+o[i]; }
__global__ void k_act(const float* g, const float* u, float* a){ int i=blockIdx.x*blockDim.x+threadIdx.x; if(i<B*F){ float x=g[i]; a[i]=x/(1.0f+expf(-x))*u[i]; } }
__global__ void k_fin(const float* h, const float* o, u16* out){ int i=blockIdx.x*blockDim.x+threadIdx.x; if(i<B*M) out[i]=f2bf(h[i]+o[i]); }

extern "C" void kernel_launch(void* const* d_in, const int* in_sizes, int n_in,
                              void* d_out, int out_size, void* d_ws, size_t ws_size,
                              hipStream_t stream) {
  const u16* x=(const u16*)d_in[0]; const u16* ckv=(const u16*)d_in[1]; const u16* wq=(const u16*)d_in[2];
  const u16* wuk=(const u16*)d_in[3]; const u16* wuv=(const u16*)d_in[4]; const u16* wo=(const u16*)d_in[5];
  const u16* wg=(const u16*)d_in[7]; const u16* wu=(const u16*)d_in[8]; const u16* wd=(const u16*)d_in[9];
  float* ws=(float*)d_ws;
  float* xn=ws; float* q=xn+2048; float* qa=q+8192; float* cc=qa+32768; float* ctx=cc+32768; float* o=ctx+8192; float* h=o+2048; float* hn=h+2048;
  float* g=hn+2048; float* u=g+16384; float* a=u+16384; float* o2=a+16384; float* sc=o2+2048;
  k_ln_x<<<2,256,0,stream>>>(x,xn);
  k_gemv<<<(H*D+63)/64,64,0,stream>>>(xn,wq,q,M,H*D);
  k_qabs<<<B*H*DC/256,256,0,stream>>>(q,wuk,qa);
  k_scores<<<dim3(SK/256,B*H),256,0,stream>>>(qa,ckv,sc);
  k_softmax<<<B*H,256,0,stream>>>(sc);
  k_pv<<<B*H,512,0,stream>>>(sc,ckv,cc);
  k_uv<<<B*H*D/256,256,0,stream>>>(cc,wuv,ctx);
  k_gemv<<<(M+63)/64,64,0,stream>>>(ctx,wo,o,H*D,M);
  k_addx<<<5,256,0,stream>>>(x,o,h);
  k_ln_f<<<2,256,0,stream>>>(h,hn);
  k_gemv<<<(F+63)/64,64,0,stream>>>(hn,wg,g,M,F);
  k_gemv<<<(F+63)/64,64,0,stream>>>(hn,wu,u,M,F);
  k_act<<<(B*F+255)/256,256,0,stream>>>(g,u,a);
  k_gemv<<<(M+63)/64,64,0,stream>>>(a,wd,o2,F,M);
  k_fin<<<5,256,0,stream>>>(h,o2,(u16*)d_out);
}
```

```cpp
#include <hip/hip_runtime.h>
#include <hip/hip_bf16.h>
#include <stdint.h>

#define MK_STAMPS 0

namespace mk {
constexpr int SK = 16384, DM = 576, DC = 512, DF = 4608;
constexpr int NWG = 256, NT = 768;
constexpr int NOWN = 72;

constexpr int WS_EPOCH = 0;
constexpr int WS_FLAG = 4096, NREP = 8;
constexpr int WS_CLR = 65536;
constexpr int WS_QA = WS_CLR;
constexpr int WS_LSE = WS_QA + 65536;
constexpr int WS_HB = WS_LSE + 131072;
constexpr int WS_OP = WS_HB + 8192;
constexpr int WS_DP = WS_OP + NOWN * 256 * 64;
constexpr int WS_CLR_END = WS_DP + NOWN * 256 * 64;
constexpr int CLR_PER_WG = (WS_CLR_END - WS_CLR) / NWG;
static_assert(CLR_PER_WG * NWG == WS_CLR_END - WS_CLR && CLR_PER_WG % 16 == 0 && CLR_PER_WG <= 10 * 1024, "clear split");
constexpr int WS_PART = 4 << 20;
constexpr int WS_STAMP = 16 << 20;
constexpr int WS_BYTES = 32 << 20;

constexpr int L_C = 0;
constexpr int L_S = 131072;
constexpr int L_CTRL = L_S + 31744;
constexpr int L_BYTES = 163840;
constexpr int L_XL = L_S;
constexpr int L_QP = L_S + 4608;
constexpr int L_QV = L_S + 4608 + 12288;
constexpr int L_P = L_S;
constexpr int L_LS = L_S + 8192;
constexpr int L_MX = L_CTRL + 64;
constexpr int L_WG = 0;
constexpr int L_CC = 4096;
constexpr int L_CU = 8192;
constexpr int L_CTX = 16384;
constexpr int L_XN2 = 17408;
constexpr int L_GU = 22528;
constexpr int L_ACT = 30720;
constexpr int L_OPL = 32768;
constexpr int L_DPL = 65536;

#define LAS __attribute__((address_space(3)))
typedef unsigned v4u __attribute__((ext_vector_type(4)));
typedef unsigned v2u __attribute__((ext_vector_type(2)));
typedef float f32x4 __attribute__((ext_vector_type(4)));
typedef float f32x2 __attribute__((ext_vector_type(2)));
typedef float f32x16 __attribute__((ext_vector_type(16)));
typedef short bf16x8 __attribute__((ext_vector_type(8)));
typedef short s16x4 __attribute__((ext_vector_type(4)));
typedef unsigned short u16;
typedef unsigned long long u64;
#define DI __device__ __forceinline__
constexpr int AUX_SC1 = 16;
constexpr unsigned SENT = 0xFFFFFFFFu;
constexpr unsigned SPIN_CAP = 1u << 16;

DI float asf(unsigned u) { return __builtin_bit_cast(float, u); }
DI unsigned asu(float f) { return __builtin_bit_cast(unsigned, f); }
DI float blo(unsigned u) { return asf(u << 16); }
DI float bhi(unsigned u) { return asf(u & 0xffff0000u); }
DI unsigned cvtpk(float lo, float hi) { unsigned r; asm("v_cvt_pk_bf16_f32 %0, %1, %2" : "=v"(r) : "v"(lo), "v"(hi)); return r; }
DI unsigned sane(unsigned u) { return u == SENT ? 0x7FC07FC0u : u; }
DI unsigned sanef(float f) { unsigned u = asu(f); return u == SENT ? 0x7FC00000u : u; }
template <int CTRL> DI float dpp(float x) { return asf((unsigned)__builtin_amdgcn_mov_dpp((int)asu(x), CTRL, 0xf, 0xf, true)); }
constexpr int XOR1 = 0xB1, XOR2 = 0x4E, HMIR = 0x141, MIR = 0x140, ROR4 = 0x124, ROR8 = 0x128;
DI float sum_row16(float x) { x += dpp<XOR1>(x); x += dpp<XOR2>(x); x += dpp<HMIR>(x); x += dpp<MIR>(x); return x; }
DI float max_row16(float x) { x = fmaxf(x, dpp<XOR1>(x)); x = fmaxf(x, dpp<XOR2>(x)); x = fmaxf(x, dpp<HMIR>(x)); x = fmaxf(x, dpp<MIR>(x)); return x; }
DI float xsum16(float x) { auto s = __builtin_amdgcn_permlane16_swap(asu(x), asu(x), false, false); return asf(s[0]) + asf(s[1]); }
DI float xsum32(float x) { auto s = __builtin_amdgcn_permlane32_swap(asu(x), asu(x), false, false); return asf(s[0]) + asf(s[1]); }
DI float xmax16(float x) { auto s = __builtin_amdgcn_permlane16_swap(asu(x), asu(x), false, false); return fmaxf(asf(s[0]), asf(s[1])); }
DI float xmax32(float x) { auto s = __builtin_amdgcn_permlane32_swap(asu(x), asu(x), false, false); return fmaxf(asf(s[0]), asf(s[1])); }
DI float tr32(float a, float b) { auto s = __builtin_amdgcn_permlane32_swap(asu(a), asu(b), false, false); return asf(s[0]) + asf(s[1]); }
DI float tr16(float a, float b) { auto s = __builtin_amdgcn_permlane16_swap(asu(a), asu(b), false, false); return asf(s[0]) + asf(s[1]); }
DI float wave_sum(float x) { return xsum32(xsum16(sum_row16(x))); }

template <int N> DI void waitv() { asm volatile("s_waitcnt vmcnt(%0)" ::"i"(N) : "memory"); }
DI void waitl() { asm volatile("s_waitcnt lgkmcnt(0)" ::: "memory"); }
DI void cfence() { asm volatile("" ::: "memory"); }
DI void bar() { asm volatile("s_waitcnt lgkmcnt(0)\n\ts_barrier" ::: "memory"); }
#define TIE(x) asm volatile("" : "+v"(x))
#define TIE8(a) asm volatile("" : "+v"(a[0]), "+v"(a[1]), "+v"(a[2]), "+v"(a[3]), "+v"(a[4]), "+v"(a[5]), "+v"(a[6]), "+v"(a[7]))

DI void gload4(v4u& d, const void* sb, unsigned vo) { asm volatile("global_load_dwordx4 %0, %1, %2" : "=v"(d) : "v"(vo), "s"(sb) : "memory"); }
DI void gload4nt(v4u& d, const void* sb, unsigned vo) { asm volatile("global_load_dwordx4 %0, %1, %2 nt" : "=v"(d) : "v"(vo), "s"(sb) : "memory"); }
DI void gload1p(unsigned& d, const void* p) { asm volatile("global_load_dword %0, %1, off" : "=v"(d) : "v"(p) : "memory"); }
DI void gloadh(unsigned& d, const void* sb, unsigned vo) { asm volatile("global_load_ushort %0, %1, %2" : "=v"(d) : "v"(vo), "s"(sb) : "memory"); }
DI void gload2sc(u64& d, const void* sb, unsigned vo) { asm volatile("global_load_dwordx2 %0, %1, %2 sc1" : "=v"(d) : "v"(vo), "s"(sb) : "memory"); }
DI void gstore4sc(const v4u& d, void* sb, unsigned vo) { asm volatile("global_store_dwordx4 %0, %1, %2 sc1\n\ts_nop 1" ::"v"(vo), "v"(d), "s"(sb) : "memory"); }
DI void gstore2sc(const u64& d, void* sb, unsigned vo) { asm volatile("global_store_dwordx2 %0, %1, %2 sc1\n\ts_nop 1" ::"v"(vo), "v"(d), "s"(sb) : "memory"); }
DI void dma16(const void* sb, unsigned vo, unsigned ldsaddr) {
  unsigned keep;
  asm volatile("s_mov_b32 %0, m0\n\ts_mov_b32 m0, %3\n\ts_nop 0\n\tglobal_load_lds_dwordx4 %1, %2 nt\n\ts_mov_b32 m0, %0"
               : "=&s"(keep) : "v"(vo), "s"(sb), "s"(ldsaddr) : "memory");
}

#define STAMP(k) do { } while (0)

DI void sig(LAS unsigned* prog, int idx, unsigned v, int lane) {
  waitl();
  if (lane == 0) *(volatile LAS unsigned*)(prog + idx) = v;
  cfence();
}
DI void wait4(LAS unsigned* prog, int first, unsigned v) {
  for (;;) {
    v4u a = *(volatile LAS v4u*)(prog + first);
    unsigned mn = min(min(a.x, a.y), min(a.z, a.w));
    if (__builtin_amdgcn_readfirstlane(mn) >= v) break;
  }
  cfence();
}
DI void wait1(LAS unsigned* prog, int idx, unsigned v) {
  for (;;) {
    unsigned a = *(volatile LAS unsigned*)(prog + idx);
    if (__builtin_amdgcn_readfirstlane(a) >= v) break;
  }
  cfence();
}
DI bool valid4(const v4u& a) { return a.x != SENT && a.y != SENT && a.z != SENT && a.w != SENT; }

DI void fma8(float (&a0)[8], float (&a1)[8], const v4u& w, float x0, float x1) {
#pragma unroll
  for (int k = 0; k < 4; ++k) {
    const float lo = blo(w[k]), hi = bhi(w[k]);
    a0[2 * k] = fmaf(lo, x0, a0[2 * k]); a1[2 * k] = fmaf(lo, x1, a1[2 * k]);
    a0[2 * k + 1] = fmaf(hi, x0, a0[2 * k + 1]); a1[2 * k + 1] = fmaf(hi, x1, a1[2 * k + 1]);
  }
}

__global__ void __launch_bounds__(NT, 3)
fused(const u16* __restrict__ x, const u16* __restrict__ ckv, const u16* __restrict__ wq, const u16* __restrict__ wuk,
      const u16* __restrict__ wuv, const u16* __restrict__ wo, const u16* __restrict__ wgt, const u16* __restrict__ wup,
      const u16* __restrict__ wdn, u16* __restrict__ out, unsigned char* __restrict__ ws) {
  extern __shared__ __attribute__((aligned(16))) unsigned char smem[];
  LAS unsigned char* L = (LAS unsigned char*)smem;
  LAS unsigned* prog = (LAS unsigned*)(L + L_CTRL);
  const int tid = threadIdx.x, lane = tid & 63, wv = __builtin_amdgcn_readfirstlane(tid >> 6);
  const int wg = blockIdx.x, xcd = wg & 7, jj = wg >> 3;
  const int h = xcd * 4 + (jj >> 3), cs = jj & 7;
  const int ab = wg >> 7, asplit = wg & 127;
  const int f0 = xcd * 576 + jj * 18;
  const bool owner = jj < 9;
  const int own = xcd * 9 + jj;
  const int rg = lane >> 4, ch = lane & 15;
  const __amdgpu_buffer_rsrc_t rs = __builtin_amdgcn_make_buffer_rsrc((void*)ws, 0, WS_BYTES, 0x00020000);
  const bool isC = wv < 4, isAdm = wv == 4;
  STAMP(0);

  if (tid < 16) prog[tid] = 0u;

  u64 ep = 0;
  if (isAdm) {
    gload2sc(ep, ws, WS_EPOCH);
    const v4u sv = {SENT, SENT, SENT, SENT};
#pragma unroll
    for (int i = 0; i < 10; ++i) {
      const int o = min(i * 1024 + lane * 16, CLR_PER_WG - 16);
      gstore4sc(sv, ws, WS_CLR + wg * CLR_PER_WG + o);
    }
  }
  v4u xa, xb, xc; unsigned xo0, xo1;
  gload4(xa, x, lane * 16);
  gload4(xb, x, 1024 + lane * 16);
  gload4(xc, x, 2048 + (lane & 15) * 16);
  gloadh(xo0, x, (48 * wv + min(lane, 47)) * 2);
  gloadh(xo1, x, 1152 + (48 * wv + min(lane, 47)) * 2);
  v4u wqv[12];
  {
    const unsigned char* b = (const unsigned char*)wq + (size_t)(48 * wv) * 8192 + h * 256;
    const unsigned vo = (12 * rg) * 8192 + ch * 16;
#pragma unroll
    for (int i = 0; i < 12; ++i) gload4(wqv[i], b + i * 8192, vo);
  }
  v4u wukv[4];
  if (isC) {
    const unsigned char* b = (const unsigned char*)wuk + (size_t)(64 * cs + 16 * wv) * 8192 + h * 256;
    const unsigned vo = (4 * rg) * 8192 + ch * 16;
#pragma unroll
    for (int i = 0; i < 4; ++i) gload4nt(wukv[i], b + i * 8192, vo);
  } else if (!isAdm) {
    const unsigned char* b = (const unsigned char*)ckv + ((size_t)ab * SK + asplit * 128) * 1024;
    const unsigned lb = (unsigned)(uintptr_t)(L + L_C);
#pragma unroll
    for (int i = 0; i < 19; ++i) {
      const int r = min(wv - 5 + 7 * i, 127);
      const int sw = ((r & 3) << 2) | ((r >> 2) & 3);
      dma16(b + r * 1024, (unsigned)(((lane & 48) | ((lane & 15) ^ sw)) * 16), lb + r * 1024);
    }
  }
  bar();
  if (isAdm) {
    waitv<17>();
    TIE(ep);
    ep += 0x9E3779B97F4A7C15ull;
    if (lane < NREP) gstore2sc(ep, ws, WS_FLAG + lane * 2048 + wg * 8);
  }
  if (isC) waitv<16>(); else if (isAdm) waitv<13>(); else waitv<31>();
  TIE(xa); TIE(xb); TIE(xc); TIE(xo0); TIE(xo1);
  STAMP(1);
  {
    float sa = 0.f, sb = 0.f, sc = 0.f;
#pragma unroll
    for (int k = 0; k < 4; ++k) { sa += blo(xa[k]) + bhi(xa[k]); sb += blo(xb[k]) + bhi(xb[k]); sc += blo(xc[k]) + bhi(xc[k]); }
    const bool b0 = lane < 8, c1 = lane < 16;
    const float m0 = wave_sum(sa + (b0 ? sb : 0.f)) * (1.0f / DM);
    const float m1 = wave_sum((b0 ? 0.f : sb) + (c1 ? sc : 0.f)) * (1.0f / DM);
    const float mb = b0 ? m0 : m1;
    float va = 0.f, vb = 0.f, vc = 0.f;
#pragma unroll
    for (int k = 0; k < 4; ++k) {
      float d;
      d = blo(xa[k]) - m0; va += d * d; d = bhi(xa[k]) - m0; va += d * d;
      d = blo(xb[k]) - mb; vb += d * d; d = bhi(xb[k]) - mb; vb += d * d;
      d = blo(xc[k]) - m1; vc += d * d; d = bhi(xc[k]) - m1; vc += d * d;
    }
    const float v0 = wave_sum(va + (b0 ? vb : 0.f)) * (1.0f / DM);
    const float v1 = wave_sum((b0 ? 0.f : vb) + (c1 ? vc : 0.f)) * (1.0f / DM);
    const float r0 = 1.0f / sqrtf(v0 + 1e-5f), r1 = 1.0f / sqrtf(v1 + 1e-5f);
    if (lane < 48) *(LAS f32x2*)(L + L_XL + (wv * 48 + lane) * 8) = f32x2{(blo(xo0) - m0) * r0, (blo(xo1) - m1) * r1};
  }
  waitl();
  {
    f32x4 xr[6];
#pragma unroll
    for (int i = 0; i < 6; ++i) xr[i] = *(LAS f32x4*)(L + L_XL + (wv * 48 + 12 * rg + 2 * i) * 8);
    float a0[8], a1[8];
#pragma unroll
    for (int k = 0; k < 8; ++k) { a0[k] = 0.f; a1[k] = 0.f; }
#pragma unroll
    for (int g = 0; g < 3; ++g) {
      if (g == 0) { if (isC) waitv<12>(); else if (isAdm) waitv<9>(); else waitv<27>(); }
      if (g == 1) { if (isC) waitv<8>(); else if (isAdm) waitv<5>(); else waitv<23>(); }
      if (g == 2) { if (isC) waitv<4>(); else if (isAdm) waitv<1>(); else waitv<19>(); }
#pragma unroll
      for (int i = 4 * g; i < 4 * g + 4; ++i) {
        TIE(wqv[i]);
        fma8(a0, a1, wqv[i], xr[i >> 1][(i & 1) * 2], xr[i >> 1][(i & 1) * 2 + 1]);
      }
    }
    float t[8];
#pragma unroll
    for (int k = 0; k < 8; ++k) t[k] = tr32(a0[k], a1[k]);
    f32x4 o;
#pragma unroll
    for (int k = 0; k < 4; ++k) o[k] = tr16(t[k], t[k + 4]);
    *(LAS f32x4*)(L + L_QP + (wv * 256 + (lane >> 5) * 128 + ch * 8 + (rg & 1) * 4) * 4) = o;
  }
  sig(prog, wv, 1u, lane);
  STAMP(2);

  if (isC) {
    wait4(prog, 0, 1u); wait4(prog, 4, 1u); wait4(prog, 8, 1u);
    {
      float s = 0.f;
#pragma unroll
      for (int w = 0; w < 12; ++w) s += *(LAS float*)(L + L_QP + (w * 256 + tid) * 4);
      *(LAS float*)(L + L_QV + tid * 4) = s;
    }
    sig(prog, wv, 2u, lane);
    wait4(prog, 0, 2u);
    {
      const f32x4 q00 = *(LAS f32x4*)(L + L_QV + (ch * 8) * 4), q01 = *(LAS f32x4*)(L + L_QV + (ch * 8 + 4) * 4);
      const f32x4 q10 = *(LAS f32x4*)(L + L_QV + (128 + ch * 8) * 4), q11 = *(LAS f32x4*)(L + L_QV + (128 + ch * 8 + 4) * 4);
      waitv<0>();
      float d0[4], d1[4];
#pragma unroll
      for (int i = 0; i < 4; ++i) {
        TIE(wukv[i]);
        const v4u w = wukv[i];
        float s0 = blo(w[0]) * q00[0], s1 = blo(w[0]) * q10[0];
        s0 = fmaf(bhi(w[0]), q00[1], s0); s1 = fmaf(bhi(w[0]), q10[1], s1);
        s0 = fmaf(blo(w[1]), q00[2], s0); s1 = fmaf(blo(w[1]), q10[2], s1);
        s0 = fmaf(bhi(w[1]), q00[3], s0); s1 = fmaf(bhi(w[1]), q10[3], s1);
        s0 = fmaf(blo(w[2]), q01[0], s0); s1 = fmaf(blo(w[2]), q11[0], s1);
        s0 = fmaf(bhi(w[2]), q01[1], s0); s1 = fmaf(bhi(w[2]), q11[1], s1);
        s0 = fmaf(blo(w[3]), q01[2], s0); s1 = fmaf(blo(w[3]), q11[2], s1);
        s0 = fmaf(bhi(w[3]), q01[3], s0); s1 = fmaf(bhi(w[3]), q11[3], s1);
        d0[i] = sum_row16(s0); d1[i] = sum_row16(s1);
      }
      constexpr float QS = 0.08838834764831845f * 1.4426950408889634f;
      const bool sel = ch & 1;
      v2u o;
      o.x = sane(cvtpk((sel ? d1[0] : d0[0]) * QS, (sel ? d1[1] : d0[1]) * QS));
      o.y = sane(cvtpk((sel ? d1[2] : d0[2]) * QS, (sel ? d1[3] : d0[3]) * QS));
      const int c = 64 * cs + 16 * wv + 4 * rg;
      const int off = WS_QA + (ch & 1) * 32768 + (c >> 4) * 1024 + (((c >> 3) & 1) * 32 + h) * 16 + (c & 7) * 2;
      STAMP(3);
      wait1(prog, 12, 1u);
      if (ch < 2) __builtin_amdgcn_raw_buffer_store_b64(o, rs, off, 0, AUX_SC1);
    }
    sig(prog, wv, 3u, lane);
    STAMP(4);
    v4u q31;
    {
      v4u qf[8];
      const int base = WS_QA + ab * 32768 + lane * 16;
      for (unsigned spin = 0; spin < SPIN_CAP; ++spin) {
        bool ok = true;
#pragma unroll
        for (int i = 0; i < 8; ++i) qf[i] = __builtin_amdgcn_raw_buffer_load_b128(rs, base + (8 * wv + i) * 1024, 0, AUX_SC1);
        q31 = __builtin_amdgcn_raw_buffer_load_b128(rs, base + 31 * 1024, 0, AUX_SC1);
#pragma unroll
        for (int i = 0; i < 8; ++i) ok = ok && valid4(qf[i]);
        ok = ok && valid4(q31);
        if (__all(ok)) break;
        __builtin_amdgcn_s_sleep(2);
      }
      wait4(prog, 0, 3u);
#pragma unroll
      for (int i = 0; i < 8; ++i)
        if (8 * wv + i < 31) *(LAS v4u*)(L + L_S + (8 * wv + i) * 1024 + lane * 16) = qf[i];
    }
    sig(prog, wv, 4u, lane);
    STAMP(5);
    wait4(prog, 0, 4u);
    wait4(prog, 4, 2u); wait4(prog, 8, 2u);
    STAMP(6);
    const int r31 = lane & 31, hh = lane >> 5;
    f32x16 sacc;
#pragma unroll
    for (int i = 0; i < 16; ++i) sacc[i] = 0.f;
    {
      const int row = 32 * wv + r31;
      const int sw = ((row & 3) << 2) | ((row >> 2) & 3);
      LAS unsigned char* arow = L + L_C + row * 1024;
#pragma unroll
      for (int s = 0; s < 32; ++s) {
        const int c64 = 2 * s + hh;
        const v4u a = *(LAS v4u*)(arow + (c64 >> 4) * 256 + (((c64 & 15) ^ sw) * 16));
        const v4u b = s < 31 ? *(LAS v4u*)(L + L_S + s * 1024 + lane * 16) : q31;
        sacc = __builtin_amdgcn_mfma_f32_32x32x16_bf16(__builtin_bit_cast(bf16x8, a), __builtin_bit_cast(bf16x8, b), sacc, 0, 0, 0);
      }
    }
    float mx = sacc[0];
#pragma unroll
    for (int i = 1; i < 16; ++i) mx = fmaxf(mx, sacc[i]);
    mx = xmax32(mx);
    if (lane < 32) *(LAS float*)(L + L_MX + (wv * 32 + lane) * 4) = mx;
    sig(prog, wv, 5u, lane);
    wait4(prog, 0, 5u);
#pragma unroll
    for (int w = 0; w < 4; ++w) mx = fmaxf(mx, *(LAS float*)(L + L_MX + (w * 32 + r31) * 4));
    {
      float ls = 0.f;
      v4u pf[2];
#pragma unroll
      for (int i = 0; i < 8; ++i) {
        const float p0 = __builtin_amdgcn_exp2f(sacc[2 * i] - mx), p1 = __builtin_amdgcn_exp2f(sacc[2 * i + 1] - mx);
        const unsigned pk = cvtpk(p0, p1);
        ls += blo(pk) + bhi(pk);
        pf[i >> 2][i & 3] = pk;
      }
      ls = xsum32(ls);
      *(LAS v4u*)(L + L_P + (wv * 2 + 0) * 1024 + lane * 16) = pf[0];
      *(LAS v4u*)(L + L_P + (wv * 2 + 1) * 1024 + lane * 16) = pf[1];
      if (lane < 32) *(LAS float*)(L + L_LS + (wv * 32 + lane) * 4) = ls;
    }
    sig(prog, wv, 6u, lane);
    wait4(prog, 0, 6u);
    STAMP(7);
    f32x16 oacc[4];
#pragma unroll
    for (int cb = 0; cb < 4; ++cb)
#pragma unroll
      for (int i = 0; i < 16; ++i) oacc[cb][i] = 0.f;
    {
      const int blk = (lane >> 4) & 1, q4 = (lane & 15) >> 2, p4 = lane & 3;
      LAS unsigned char* tb = L + L_C + (4 * hh + q4) * 1024 + wv * 256 + 8 * (p4 & 1);
#pragma unroll
      for (int ks = 0; ks < 8; ++ks) {
        const v4u pb = *(LAS v4u*)(L + L_P + ks * 1024 + lane * 16);
#pragma unroll
        for (int cb = 0; cb < 4; ++cb) {
          s16x4 a[2];
#pragma unroll
          for (int t = 0; t < 2; ++t) {
            const int chunk = ((cb ^ q4) << 2) | ((2 * blk + (p4 >> 1)) ^ (2 * t + hh));
            a[t] = __builtin_amdgcn_ds_read_tr16_b64_v4i16((LAS s16x4*)(tb + (16 * ks + 8 * t) * 1024 + chunk * 16));
          }
          const bf16x8 A = __builtin_shufflevector(a[0], a[1], 0, 1, 2, 3, 4, 5, 6, 7);
          oacc[cb] = __builtin_amdgcn_mfma_f32_32x32x16_bf16(A, __builtin_bit_cast(bf16x8, pb), oacc[cb], 0, 0, 0);
        }
      }
    }
    {
      float lt = 0.f;
#pragma unroll
      for (int w = 0; w < 4; ++w) lt += *(LAS float*)(L + L_LS + (w * 32 + r31) * 4);
      const float inv = 1.0f / lt;
#pragma unroll
      for (int cb = 0; cb < 4; ++cb) {
#pragma unroll
        for (int g = 0; g < 4; g += 2) {
          unsigned a0 = cvtpk(oacc[cb][4 * g] * inv, oacc[cb][4 * g + 1] * inv), a1 = cvtpk(oacc[cb][4 * g + 2] * inv, oacc[cb][4 * g + 3] * inv);
          unsigned b0 = cvtpk(oacc[cb][4 * g + 4] * inv, oacc[cb][4 * g + 5] * inv), b1 = cvtpk(oacc[cb][4 * g + 6] * inv, oacc[cb][4 * g + 7] * inv);
          auto s0 = __builtin_amdgcn_permlane32_swap(a0, b0, false, false);
          auto s1 = __builtin_amdgcn_permlane32_swap(a1, b1, false, false);
          const v4u o = {s0[0], s1[0], s0[1], s1[1]};
          const int cl = 32 * cb + 8 * (g + hh);
          const int off = WS_PART + ((((r31 * 8 + 2 * wv + (cl >> 6)) * 2 + ab) * 128 + asplit) * 64 + (cl & 63)) * 2;
          __builtin_amdgcn_raw_buffer_store_b128(o, rs, off, 0, AUX_SC1);
        }
      }
      STAMP(8);
      waitv<0>();
      if (lane < 32) {
        const float lse = mx + __builtin_amdgcn_logf(lt);
        __builtin_amdgcn_raw_buffer_store_b32(sanef(lse), rs, WS_LSE + (((lane * 4 + wv) * 2 + ab) * 128 + asplit) * 4, 0, AUX_SC1);
      }
    }
    STAMP(9);
    {
      v4u lv;
      for (unsigned spin = 0; spin < SPIN_CAP; ++spin) {
        lv = __builtin_amdgcn_raw_buffer_load_b128(rs, WS_LSE + (h * 4 + (cs >> 1)) * 1024 + lane * 16, 0, AUX_SC1);
        if (__all(valid4(lv))) break;
        __builtin_amdgcn_s_sleep(2);
      }
      cfence();
      STAMP(10);
      v4u pv[8];
      const int pbase = WS_PART + (((h * 8 + cs) * 2 + (wv >> 1)) * 128 + 64 * (wv & 1)) * 128 + lane * 16;
#pragma unroll
      for (int i = 0; i < 8; ++i) pv[i] = __builtin_amdgcn_raw_buffer_load_b128(rs, pbase + i * 1024, 0, AUX_SC1);
      if (wg == 0 && wv == 0 && lane == 0) {
        u64 e = __hip_atomic_load((u64*)(ws + WS_EPOCH), __ATOMIC_RELAXED, __HIP_MEMORY_SCOPE_AGENT);
        __hip_atomic_store((u64*)(ws + WS_EPOCH), e + 1ull, __ATOMIC_RELAXED, __HIP_MEMORY_SCOPE_AGENT);
      }
      float m = fmaxf(fmaxf(asf(lv.x), asf(lv.y)), fmaxf(asf(lv.z), asf(lv.w)));
      m = xmax16(max_row16(m));
      f32x4 e;
#pragma unroll
      for (int k = 0; k < 4; ++k) e[k] = __builtin_amdgcn_exp2f(asf(lv[k]) - m);
      const float tot = xsum16(sum_row16((e[0] + e[1]) + (e[2] + e[3])));
      const float it = 1.0f / tot;
      *(LAS f32x4*)(L + L_WG + wv * 1024 + lane * 16) = e * it;
      waitl();
      float acc[8];
#pragma unroll
      for (int k = 0; k < 8; ++k) acc[k] = 0.f;
#pragma unroll
      for (int i = 0; i < 8; ++i) {
        const float w = *(LAS float*)(L + L_WG + wv * 1024 + ((wv >> 1) * 128 + 64 * (wv & 1) + 8 * i + (lane >> 3)) * 4);
#pragma unroll
        for (int k = 0; k < 4; ++k) { acc[2 * k] = fmaf(w, blo(pv[i][k]), acc[2 * k]); acc[2 * k + 1] = fmaf(w, bhi(pv[i][k]), acc[2 * k + 1]); }
      }
      float t[4], u2[2];
#pragma unroll
      for (int k = 0; k < 4; ++k) t[k] = tr32(acc[k], acc[k + 4]);
#pragma unroll
      for (int k = 0; k < 2; ++k) u2[k] = tr16(t[k], t[k + 2]);
      u2[0] += dpp<ROR8>(u2[0]); u2[1] += dpp<ROR8>(u2[1]);
      if ((lane & 8) == 0) *(LAS f32x2*)(L + L_CC + (wv * 64 + (lane & 7) * 8 + (lane >> 5) * 4 + ((lane >> 4) & 1) * 2) * 4) = f32x2{u2[0], u2[1]};
    }
    STAMP(11);
    bar();
    bar();
    {
      float s = 0.f;
#pragma unroll
      for (int w = 0; w < 8; ++w) s += *(LAS float*)(L + L_CU + (w * 256 + tid) * 4);
      *(LAS float*)(L + L_CTX + ((tid & 127) * 2 + (tid >> 7)) * 4) = s;
    }
    bar();
    bar();
    STAMP(12);
    f32x4 hown = {0.f, 0.f, 0.f, 0.f};
    if (wv == 1 && owner) {
      const v2u xv = *(const v2u*)(x + own * 16 + (lane & 3) * 4);
      v4u pv[16];
      for (unsigned spin = 0; spin < SPIN_CAP; ++spin) {
        bool ok = true;
#pragma unroll
        for (int i = 0; i < 16; ++i) pv[i] = __builtin_amdgcn_raw_buffer_load_b128(rs, WS_OP + own * 16384 + i * 1024 + lane * 16, 0, AUX_SC1);
#pragma unroll
        for (int i = 0; i < 16; ++i) ok = ok && valid4(pv[i]);
        if (__all(ok)) break;
        __builtin_amdgcn_s_sleep(1);
      }
      f32x4 s = {0.f, 0.f, 0.f, 0.f};
#pragma unroll
      for (int i = 0; i < 16; ++i) s += __builtin_bit_cast(f32x4, pv[i]);
#pragma unroll
      for (int k = 0; k < 4; ++k) { float t = s[k]; t += dpp<ROR8>(t); t += dpp<ROR4>(t); s[k] = xsum32(xsum16(t)); }
      hown = f32x4{blo(xv.x) + s[0], bhi(xv.x) + s[1], blo(xv.y) + s[2], bhi(xv.y) + s[3]};
      const v4u o = {sanef(hown[0]), sanef(hown[1]), sanef(hown[2]), sanef(hown[3])};
      if (lane < 4) __builtin_amdgcn_raw_buffer_store_b128(o, rs, WS_HB + own * 64 + lane * 16, 0, AUX_SC1);
      STAMP(13);
    }
    if (wv == 0) {
      v4u hv[5];
      for (unsigned spin = 0; spin < SPIN_CAP; ++spin) {
        bool ok = true;
#pragma unroll
        for (int k = 0; k < 4; ++k) hv[k] = __builtin_amdgcn_raw_buffer_load_b128(rs, WS_HB + k * 1024 + lane * 16, 0, AUX_SC1);
        hv[4] = __builtin_amdgcn_raw_buffer_load_b128(rs, WS_HB + 4096 + (lane & 31) * 16, 0, AUX_SC1);
#pragma unroll
        for (int k = 0; k < 5; ++k) ok = ok && valid4(hv[k]);
        if (__all(ok)) break;
        __builtin_amdgcn_s_sleep(1);
      }
      STAMP(13);
      f32x4 f[5];
#pragma unroll
      for (int k = 0; k < 5; ++k) f[k] = __builtin_bit_cast(f32x4, hv[k]);
      const bool m2 = lane < 16, m4 = lane < 32;
      float s[5];
#pragma unroll
      for (int k = 0; k < 5; ++k) s[k] = (f[k][0] + f[k][1]) + (f[k][2] + f[k][3]);
      const float m0 = wave_sum(s[0] + s[1] + (m2 ? s[2] : 0.f)) * (1.0f / DM);
      const float m1 = wave_sum((m2 ? 0.f : s[2]) + s[3] + (m4 ? s[4] : 0.f)) * (1.0f / DM);
      const float mm[5] = {m0, m0, m2 ? m0 : m1, m1, m1};
#pragma unroll
      for (int k = 0; k < 5; ++k) { f[k] -= mm[k]; s[k] = (f[k][0] * f[k][0] + f[k][1] * f[k][1]) + (f[k][2] * f[k][2] + f[k][3] * f[k][3]); }
      const float v0 = wave_sum(s[0] + s[1] + (m2 ? s[2] : 0.f)) * (1.0f / DM);
      const float v1 = wave_sum((m2 ? 0.f : s[2]) + s[3] + (m4 ? s[4] : 0.f)) * (1.0f / DM);
      const float r0 = 1.0f / sqrtf(v0 + 1e-5f), r1 = 1.0f / sqrtf(v1 + 1e-5f);
      const float rr[5] = {r0, r0, m2 ? r0 : r1, r1, r1};
#pragma unroll
      for (int k = 0; k < 5; ++k) {
        const int i4 = 64 * k + lane;
        if (k < 4 || m4) {
          const int b = i4 >= 144 ? 1 : 0, m = (i4 - 144 * b) * 4;
#pragma unroll
          for (int e = 0; e < 4; ++e) *(LAS float*)(L + L_XN2 + ((m + e) * 2 + b) * 4) = f[k][e] * rr[k];
        }
      }
    }
    bar();
    bar();
    if (tid < 72) {
      const int dw = tid >> 3, mat = (tid >> 2) & 1, gq = tid & 3;
      f32x4 s = {0.f, 0.f, 0.f, 0.f};
#pragma unroll
      for (int k = 0; k < 7; ++k) s += *(LAS f32x4*)(L + L_GU + ((7 * gq + k) * 18 + mat * 9 + dw) * 16);
      f32x4 o;
#pragma unroll
      for (int k = 0; k < 4; ++k) {
        float t = s[k]; t += dpp<XOR1>(t); t += dpp<XOR2>(t);
        const float u = dpp<HMIR>(t);
        o[k] = t / (1.0f + __expf(-t)) * u;
      }
      if ((tid & 7) == 0) *(LAS f32x4*)(L + L_ACT + dw * 16) = o;
    }
    bar();
    bar();
    STAMP(14);
    if (wv == 1 && owner) {
      v4u pv[16];
      for (unsigned spin = 0; spin < SPIN_CAP; ++spin) {
        bool ok = true;
#pragma unroll
        for (int i = 0; i < 16; ++i) pv[i] = __builtin_amdgcn_raw_buffer_load_b128(rs, WS_DP + own * 16384 + i * 1024 + lane * 16, 0, AUX_SC1);
#pragma unroll
        for (int i = 0; i < 16; ++i) ok = ok && valid4(pv[i]);
        if (__all(ok)) break;
        __builtin_amdgcn_s_sleep(1);
      }
      f32x4 s = {0.f, 0.f, 0.f, 0.f};
#pragma unroll
      for (int i = 0; i < 16; ++i) s += __builtin_bit_cast(f32x4, pv[i]);
#pragma unroll
      for (int k = 0; k < 4; ++k) { float t = s[k]; t += dpp<ROR8>(t); t += dpp<ROR4>(t); s[k] = xsum32(xsum16(t)); }
      v2u o;
      o.x = cvtpk(hown[0] + s[0], hown[1] + s[1]);
      o.y = cvtpk(hown[2] + s[2], hown[3] + s[3]);
      if (lane < 4) *(v2u*)(out + own * 16 + lane * 4) = o;
      STAMP(15);
    }
  } else {
    v4u wuvr[2], wor[19], wdr[3]; unsigned wgur[21];
    const int td = tid - 256;
    if (isAdm) {
      const unsigned elo = (unsigned)ep, ehi = (unsigned)(ep >> 32);
      for (unsigned spin = 0; spin < SPIN_CAP; ++spin) {
        const v4u a = __builtin_amdgcn_raw_buffer_load_b128(rs, WS_FLAG + (wg & (NREP - 1)) * 2048 + lane * 32, 0, AUX_SC1);
        const v4u b = __builtin_amdgcn_raw_buffer_load_b128(rs, WS_FLAG + (wg & (NREP - 1)) * 2048 + lane * 32 + 16, 0, AUX_SC1);
        const bool ok = a.x == elo && a.y == ehi && a.z == elo && a.w == ehi && b.x == elo && b.y == ehi && b.z == elo && b.w == ehi;
        if (__all(ok)) break;
        __builtin_amdgcn_s_sleep(2);
      }
      waitv<0>();
      sig(prog, 12, 1u, lane);
      sig(prog, 4, 2u, lane);
      STAMP(3);
      wait1(prog, 13, 1u);
    } else {
      waitv<10>();
      if (wv == 5) sig(prog, 13, 1u, lane);
    }
    const int wd = wv - 4;
    const int og = min(td / 72, 6), oc = td % 72;
    {
      const unsigned char* b = (const unsigned char*)wuv + (size_t)(64 * cs + 8 * wd) * 8192 + h * 256;
      gload4nt(wuvr[0], b, rg * 8192 + ch * 16);
      gload4nt(wuvr[1], b + 4 * 8192, rg * 8192 + ch * 16);
    }
    const unsigned char* wob = (const unsigned char*)wo + (size_t)(h * 128) * 1152;
    const unsigned wovo = og * 1152 + oc * 16;
#pragma unroll
    for (int i = 0; i < 8; ++i) gload4(wor[i], wob + 7 * i * 1152, wovo);
    if (!isAdm) { waitv<10>(); sig(prog, wv, 2u, lane); STAMP(3); }
#pragma unroll
    for (int i = 8; i < 18; ++i) gload4(wor[i], wob + 7 * i * 1152, wovo);
    gload4(wor[18], wob, min(126 + og, 127) * 1152 + oc * 16);
    waitv<10>();
    const int gu = td % 18, gg = min(td / 18, 27);
    const int dg = min(td / 72, 5);
    {
      const unsigned char* p = (const unsigned char*)(gu < 9 ? wgt : wup) + (size_t)gg * 9216 + f0 * 2 + (gu % 9) * 4;
#pragma unroll
      for (int i = 0; i < 20; ++i) {
        if (i == 10) waitv<10>();
        gload1p(wgur[i], p + (size_t)i * 28 * 9216);
      }
      gload1p(wgur[20], p + (size_t)(gg < 16 ? 560 : 0) * 9216);
    }
    {
      const unsigned char* b = (const unsigned char*)wdn + (size_t)f0 * 1152;
#pragma unroll
      for (int i = 0; i < 3; ++i) gload4nt(wdr[i], b + i * 1152, dg * 3 * 1152 + oc * 16);
    }
    STAMP(4);
    bar();
    {
      waitv<43>();
      TIE(wuvr[0]); TIE(wuvr[1]);
      float a0[8], a1[8];
#pragma unroll
      for (int k = 0; k < 8; ++k) { a0[k] = 0.f; a1[k] = 0.f; }
#pragma unroll
      for (int i = 0; i < 2; ++i) {
        const int c = 8 * wd + 4 * i + rg;
        const float x0 = *(LAS float*)(L + L_CC + c * 4) + *(LAS float*)(L + L_CC + (64 + c) * 4);
        const float x1 = *(LAS float*)(L + L_CC + (128 + c) * 4) + *(LAS float*)(L + L_CC + (192 + c) * 4);
        fma8(a0, a1, wuvr[i], x0, x1);
      }
      float t[8];
#pragma unroll
      for (int k = 0; k < 8; ++k) t[k] = tr32(a0[k], a1[k]);
      f32x4 o;
#pragma unroll
      for (int k = 0; k < 4; ++k) o[k] = tr16(t[k], t[k + 4]);
      *(LAS f32x4*)(L + L_CU + (wd * 256 + (lane >> 5) * 128 + ch * 8 + (rg & 1) * 4) * 4) = o;
    }
    bar();
    bar();
    {
      waitv<24>();
      float a0[8], a1[8];
#pragma unroll
      for (int k = 0; k < 8; ++k) { a0[k] = 0.f; a1[k] = 0.f; }
#pragma unroll
      for (int i = 0; i < 19; ++i) {
        TIE(wor[i]);
        const int d = og + 7 * i;
        f32x2 xv = *(LAS f32x2*)(L + L_CTX + min(d, 127) * 8);
        if (d > 127) xv = f32x2{0.f, 0.f};
        fma8(a0, a1, wor[i], xv[0], xv[1]);
        if ((i % 4) == 3 || i == 18) { TIE8(a0); TIE8(a1); }
      }
      if (td < 504) {
        LAS float* o = (LAS float*)(L + L_OPL + (og * 1152 + oc * 8) * 4);
        *(LAS f32x4*)(o) = f32x4{a0[0], a0[1], a0[2], a0[3]};
        *(LAS f32x4*)(o + 4) = f32x4{a0[4], a0[5], a0[6], a0[7]};
        *(LAS f32x4*)(o + 576) = f32x4{a1[0], a1[1], a1[2], a1[3]};
        *(LAS f32x4*)(o + 580) = f32x4{a1[4], a1[5], a1[6], a1[7]};
      }
    }
    bar();
    if (td < 288) {
      f32x4 s = {0.f, 0.f, 0.f, 0.f};
#pragma unroll
      for (int g = 0; g < 7; ++g) s += *(LAS f32x4*)(L + L_OPL + (g * 1152 + td * 4) * 4);
      const v4u o = {sanef(s[0]), sanef(s[1]), sanef(s[2]), sanef(s[3])};
      __builtin_amdgcn_raw_buffer_store_b128(o, rs, WS_OP + ((td >> 2) * 256 + wg) * 64 + (td & 3) * 16, 0, AUX_SC1);
    }
    STAMP(12);
    bar();
    {
      waitv<3>();
      f32x4 a = {0.f, 0.f, 0.f, 0.f};
#pragma unroll
      for (int i = 0; i < 21; ++i) {
        TIE(wgur[i]);
        const int m = gg + 28 * i;
        f32x2 xv = *(LAS f32x2*)(L + L_XN2 + min(m, 575) * 8);
        if (m > 575) xv = f32x2{0.f, 0.f};
        const float lo = blo(wgur[i]), hi = bhi(wgur[i]);
        a[0] = fmaf(lo, xv[0], a[0]); a[1] = fmaf(lo, xv[1], a[1]); a[2] = fmaf(hi, xv[0], a[2]); a[3] = fmaf(hi, xv[1], a[3]);
      }
      TIE(a);
      if (td < 504) *(LAS f32x4*)(L + L_GU + (gg * 18 + gu) * 16) = a;
    }
    bar();
    bar();
    {
      waitv<0>();
      float a0[8], a1[8];
#pragma unroll
      for (int k = 0; k < 8; ++k) { a0[k] = 0.f; a1[k] = 0.f; }
#pragma unroll
      for (int i = 0; i < 3; ++i) {
        TIE(wdr[i]);
        const f32x2 xv = *(LAS f32x2*)(L + L_ACT + (3 * dg + i) * 8);
        fma8(a0, a1, wdr[i], xv[0], xv[1]);
      }
      TIE8(a0); TIE8(a1);
      if (td < 432) {
        LAS float* o = (LAS float*)(L + L_DPL + (dg * 1152 + oc * 8) * 4);
        *(LAS f32x4*)(o) = f32x4{a0[0], a0[1], a0[2], a0[3]};
        *(LAS f32x4*)(o + 4) = f32x4{a0[4], a0[5], a0[6], a0[7]};
        *(LAS f32x4*)(o + 576) = f32x4{a1[0], a1[1], a1[2], a1[3]};
        *(LAS f32x4*)(o + 580) = f32x4{a1[4], a1[5], a1[6], a1[7]};
      }
    }
    bar();
    if (td < 288) {
      f32x4 s = {0.f, 0.f, 0.f, 0.f};
#pragma unroll
      for (int g = 0; g < 6; ++g) s += *(LAS f32x4*)(L + L_DPL + (g * 1152 + td * 4) * 4);
      const v4u o = {sanef(s[0]), sanef(s[1]), sanef(s[2]), sanef(s[3])};
      __builtin_amdgcn_raw_buffer_store_b128(o, rs, WS_DP + ((td >> 2) * 256 + wg) * 64 + (td & 3) * 16, 0, AUX_SC1);
    }
    STAMP(14);
  }
}
}

extern "C" void kernel_launch(void* const* d_in, const int* in_sizes, int n_in,
                              void* d_out, int out_size, void* d_ws, size_t ws_size,
                              hipStream_t stream) {
  using mk::u16;
  hipFuncSetAttribute((const void*)mk::fused, hipFuncAttributeMaxDynamicSharedMemorySize, mk::L_BYTES);
  mk::fused<<<mk::NWG, mk::NT, mk::L_BYTES, stream>>>(
      (const u16*)d_in[0], (const u16*)d_in[1], (const u16*)d_in[2], (const u16*)d_in[3], (const u16*)d_in[4], (const u16*)d_in[5],
      (const u16*)d_in[7], (const u16*)d_in[8], (const u16*)d_in[9], (u16*)d_out, (unsigned char*)d_ws);
}
```

```cpp
#include <hip/hip_runtime.h>
#include <hip/hip_bf16.h>
#include <stdint.h>

#define MK_STAMPS 0

namespace mk {
constexpr int SK = 16384, DM = 576, DC = 512, DF = 4608;
constexpr int NWG = 256, NT = 768;
constexpr int NOWN = 72;

constexpr int WS_EPOCH = 0;
constexpr int WS_FLAG = 4096, NREP = 8;
constexpr int WS_CLR = 65536;
constexpr int WS_QA = WS_CLR;
constexpr int WS_LSE = WS_QA + 65536;
constexpr int WS_HB = WS_LSE + 131072;
constexpr int WS_Q0 = WS_HB + 8192;
constexpr int WS_CX = WS_Q0 + 262144;
constexpr int WS_OP = WS_CX + 262144;
constexpr int WS_DP = WS_OP + NOWN * 32 * 64;
constexpr int WS_CLR_END = WS_DP + NOWN * 256 * 64;
constexpr int CLR_PER_WG = (WS_CLR_END - WS_CLR) / NWG;
static_assert(CLR_PER_WG * NWG == WS_CLR_END - WS_CLR && CLR_PER_WG % 16 == 0 && CLR_PER_WG <= 8 * 1024, "clear split");
constexpr int WS_PART = 4 << 20;
constexpr int WS_STAMP = 16 << 20;
constexpr int WS_BYTES = 32 << 20;

constexpr int L_C = 0;
constexpr int L_S = 131072;
constexpr int L_CTRL = L_S + 31744;
constexpr int L_BYTES = 163840;
constexpr int L_XL = L_S;
constexpr int L_QP = L_S + 1024;
constexpr int L_QV = L_S + 8192;
constexpr int L_P = L_S;
constexpr int L_LS = L_S + 8192;
constexpr int L_MX = L_CTRL + 64;
constexpr int L_WG = 0;
constexpr int L_CC = 4096;
constexpr int L_CU = 8192;
constexpr int L_CTX = 16384;
constexpr int L_XN2 = 17408;
constexpr int L_GU = 22528;
constexpr int L_ACT = 30720;
constexpr int L_OPL = 32768;
constexpr int L_DPL = 69632;

#define LAS __attribute__((address_space(3)))
typedef unsigned v4u __attribute__((ext_vector_type(4)));
typedef unsigned v2u __attribute__((ext_vector_type(2)));
typedef float f32x4 __attribute__((ext_vector_type(4)));
typedef float f32x2 __attribute__((ext_vector_type(2)));
typedef float f32x16 __attribute__((ext_vector_type(16)));
typedef short bf16x8 __attribute__((ext_vector_type(8)));
typedef short s16x4 __attribute__((ext_vector_type(4)));
typedef unsigned short u16;
typedef unsigned long long u64;
#define DI __device__ __forceinline__
constexpr int AUX_SC1 = 16;
constexpr unsigned SENT = 0xFFFFFFFFu;
constexpr unsigned SPIN_CAP = 1u << 16;

DI float asf(unsigned u) { return __builtin_bit_cast(float, u); }
DI unsigned asu(float f) { return __builtin_bit_cast(unsigned, f); }
DI float blo(unsigned u) { return asf(u << 16); }
DI float bhi(unsigned u) { return asf(u & 0xffff0000u); }
DI unsigned cvtpk(float lo, float hi) { unsigned r; asm("v_cvt_pk_bf16_f32 %0, %1, %2" : "=v"(r) : "v"(lo), "v"(hi)); return r; }
DI unsigned sane(unsigned u) { return u == SENT ? 0x7FC07FC0u : u; }
DI unsigned sanef(float f) { unsigned u = asu(f); return u == SENT ? 0x7FC00000u : u; }
template <int CTRL> DI float dpp(float x) { return asf((unsigned)__builtin_amdgcn_mov_dpp((int)asu(x), CTRL, 0xf, 0xf, true)); }
constexpr int XOR1 = 0xB1, XOR2 = 0x4E, HMIR = 0x141, MIR = 0x140, ROR4 = 0x124, ROR8 = 0x128;
DI float sum_row16(float x) { x += dpp<XOR1>(x); x += dpp<XOR2>(x); x += dpp<HMIR>(x); x += dpp<MIR>(x); return x; }
DI float max_row16(float x) { x = fmaxf(x, dpp<XOR1>(x)); x = fmaxf(x, dpp<XOR2>(x)); x = fmaxf(x, dpp<HMIR>(x)); x = fmaxf(x, dpp<MIR>(x)); return x; }
DI float xsum16(float x) { auto s = __builtin_amdgcn_permlane16_swap(asu(x), asu(x), false, false); return asf(s[0]) + asf(s[1]); }
DI float xsum32(float x) { auto s = __builtin_amdgcn_permlane32_swap(asu(x), asu(x), false, false); return asf(s[0]) + asf(s[1]); }
DI float xmax16(float x) { auto s = __builtin_amdgcn_permlane16_swap(asu(x), asu(x), false, false); return fmaxf(asf(s[0]), asf(s[1])); }
DI float xmax32(float x) { auto s = __builtin_amdgcn_permlane32_swap(asu(x), asu(x), false, false); return fmaxf(asf(s[0]), asf(s[1])); }
DI float tr32(float a, float b) { auto s = __builtin_amdgcn_permlane32_swap(asu(a), asu(b), false, false); return asf(s[0]) + asf(s[1]); }
DI float tr16(float a, float b) { auto s = __builtin_amdgcn_permlane16_swap(asu(a), asu(b), false, false); return asf(s[0]) + asf(s[1]); }
DI float wave_sum(float x) { return xsum32(xsum16(sum_row16(x))); }

template <int N> DI void waitv() { asm volatile("s_waitcnt vmcnt(%0)" ::"i"(N) : "memory"); }
DI void waitl() { asm volatile("s_waitcnt lgkmcnt(0)" ::: "memory"); }
DI void cfence() { asm volatile("" ::: "memory"); }
DI void bar() { asm volatile("s_waitcnt lgkmcnt(0)\n\ts_barrier" ::: "memory"); }
#define TIE(x) asm volatile("" : "+v"(x))
#define TIE8(a) asm volatile("" : "+v"(a[0]), "+v"(a[1]), "+v"(a[2]), "+v"(a[3]), "+v"(a[4]), "+v"(a[5]), "+v"(a[6]), "+v"(a[7]))

DI void gload4(v4u& d, const void* sb, unsigned vo) { asm volatile("global_load_dwordx4 %0, %1, %2" : "=v"(d) : "v"(vo), "s"(sb) : "memory"); }
DI void gload4nt(v4u& d, const void* sb, unsigned vo) { asm volatile("global_load_dwordx4 %0, %1, %2 nt" : "=v"(d) : "v"(vo), "s"(sb) : "memory"); }
DI void gload1p(unsigned& d, const void* p) { asm volatile("global_load_dword %0, %1, off nt" : "=v"(d) : "v"(p) : "memory"); }
DI void gloadh(unsigned& d, const void* sb, unsigned vo) { asm volatile("global_load_ushort %0, %1, %2" : "=v"(d) : "v"(vo), "s"(sb) : "memory"); }
DI void gload2sc(u64& d, const void* sb, unsigned vo) { asm volatile("global_load_dwordx2 %0, %1, %2 sc1" : "=v"(d) : "v"(vo), "s"(sb) : "memory"); }
DI void gstore4sc(const v4u& d, void* sb, unsigned vo) { asm volatile("global_store_dwordx4 %0, %1, %2 sc1\n\ts_nop 1" ::"v"(vo), "v"(d), "s"(sb) : "memory"); }
DI void gstore2sc(const u64& d, void* sb, unsigned vo) { asm volatile("global_store_dwordx2 %0, %1, %2 sc1\n\ts_nop 1" ::"v"(vo), "v"(d), "s"(sb) : "memory"); }
DI void dma16(const void* sb, unsigned vo, unsigned ldsaddr) {
  unsigned keep;
  asm volatile("s_mov_b32 %0, m0\n\ts_mov_b32 m0, %3\n\ts_nop 0\n\tglobal_load_lds_dwordx4 %1, %2 nt\n\ts_mov_b32 m0, %0"
               : "=&s"(keep) : "v"(vo), "s"(sb), "s"(ldsaddr) : "memory");
}

#define STAMP(k) do { } while (0)

DI void sig(LAS unsigned* prog, int idx, unsigned v, int lane) {
  waitl();
  if (lane == 0) *(volatile LAS unsigned*)(prog + idx) = v;
  cfence();
}
DI void wait4(LAS unsigned* prog, int first, unsigned v) {
  for (;;) {
    v4u a = *(volatile LAS v4u*)(prog + first);
    unsigned mn = min(min(a.x, a.y), min(a.z, a.w));
    if (__builtin_amdgcn_readfirstlane(mn) >= v) break;
  }
  cfence();
}
DI void wait1(LAS unsigned* prog, int idx, unsigned v) {
  for (;;) {
    unsigned a = *(volatile LAS unsigned*)(prog + idx);
    if (__builtin_amdgcn_readfirstlane(a) >= v) break;
  }
  cfence();
}
DI bool valid4(const v4u& a) { return a.x != SENT && a.y != SENT && a.z != SENT && a.w != SENT; }

DI void fma8(float (&a0)[8], float (&a1)[8], const v4u& w, float x0, float x1) {
#pragma unroll
  for (int k = 0; k < 4; ++k) {
    const float lo = blo(w[k]), hi = bhi(w[k]);
    a0[2 * k] = fmaf(lo, x0, a0[2 * k]); a1[2 * k] = fmaf(lo, x1, a1[2 * k]);
    a0[2 * k + 1] = fmaf(hi, x0, a0[2 * k + 1]); a1[2 * k + 1] = fmaf(hi, x1, a1[2 * k + 1]);
  }
}

template <int N> DI f32x4 own_reduce(__amdgpu_buffer_rsrc_t rs, int base, int lane) {
  v4u pv[N];
  for (unsigned spin = 0; spin < SPIN_CAP; ++spin) {
    bool ok = true;
#pragma unroll
    for (int i = 0; i < N; ++i) pv[i] = __builtin_amdgcn_raw_buffer_load_b128(rs, base + i * 1024 + lane * 16, 0, AUX_SC1);
#pragma unroll
    for (int i = 0; i < N; ++i) ok = ok && valid4(pv[i]);
    if (__all(ok)) break;
    __builtin_amdgcn_s_sleep(1);
  }
  f32x4 s = __builtin_bit_cast(f32x4, pv[0]);
#pragma unroll
  for (int i = 1; i < N; ++i) s += __builtin_bit_cast(f32x4, pv[i]);
#pragma unroll
  for (int k = 0; k < 4; ++k) { float t = s[k]; t += dpp<ROR8>(t); t += dpp<ROR4>(t); s[k] = xsum32(xsum16(t)); }
  return s;
}
DI f32x4 gather8(__amdgpu_buffer_rsrc_t rs, int base, int lane) {
  v4u pv[8];
  for (unsigned spin = 0; spin < SPIN_CAP; ++spin) {
    bool ok = true;
#pragma unroll
    for (int i = 0; i < 8; ++i) pv[i] = __builtin_amdgcn_raw_buffer_load_b128(rs, base + i * 1024 + lane * 16, 0, AUX_SC1);
#pragma unroll
    for (int i = 0; i < 8; ++i) ok = ok && valid4(pv[i]);
    if (__all(ok)) break;
    __builtin_amdgcn_s_sleep(1);
  }
  f32x4 s = __builtin_bit_cast(f32x4, pv[0]);
#pragma unroll
  for (int i = 1; i < 8; ++i) s += __builtin_bit_cast(f32x4, pv[i]);
  return s;
}
DI void publish4(__amdgpu_buffer_rsrc_t rs, int off, const f32x4& s) {
  const v4u o = {sanef(s[0]), sanef(s[1]), sanef(s[2]), sanef(s[3])};
  __builtin_amdgcn_raw_buffer_store_b128(o, rs, off, 0, AUX_SC1);
}

__global__ void __launch_bounds__(NT, 3)
fused(const u16* __restrict__ x, const u16* __restrict__ ckv, const u16* __restrict__ wq, const u16* __restrict__ wuk,
      const u16* __restrict__ wuv, const u16* __restrict__ wo, const u16* __restrict__ wgt, const u16* __restrict__ wup,
      const u16* __restrict__ wdn, u16* __restrict__ out, unsigned char* __restrict__ ws) {
  extern __shared__ __attribute__((aligned(16))) unsigned char smem[];
  LAS unsigned char* L = (LAS unsigned char*)smem;
  LAS unsigned* prog = (LAS unsigned*)(L + L_CTRL);
  const int tid = threadIdx.x, lane = tid & 63, wv = __builtin_amdgcn_readfirstlane(tid >> 6);
  const int wg = blockIdx.x, xcd = wg & 7, jj = wg >> 3;
  const int h = xcd * 4 + (jj >> 3), cs = jj & 7;
  const int ab = wg >> 7, asplit = wg & 127;
  const int f0 = xcd * 576 + jj * 18;
  const bool owner = jj < 9;
  const int own = xcd * 9 + jj;
  const int rg = lane >> 4, ch = lane & 15;
  const __amdgpu_buffer_rsrc_t rs = __builtin_amdgcn_make_buffer_rsrc((void*)ws, 0, WS_BYTES, 0x00020000);
  const bool isC = wv < 4, isAdm = wv == 4, isDma = wv >= 8;
  const bool isA = wv < 7 && wv != 4;
  const int aw = wv < 4 ? wv : wv - 1;
  STAMP(0);
  if (tid < 16) prog[tid] = 0u;

  u64 ep = 0;
  v4u xa, xb, xc, wqv[3], wukv[4]; unsigned xo0, xo1;
  if (isAdm) {
    gload2sc(ep, ws, WS_EPOCH);
    const v4u sv = {SENT, SENT, SENT, SENT};
#pragma unroll
    for (int i = 0; i < 8; ++i) gstore4sc(sv, ws, WS_CLR + wg * CLR_PER_WG + min(i * 1024 + lane * 16, CLR_PER_WG - 16));
  } else if (isA) {
    const int m0 = 72 * cs + 12 * aw;
    gload4(xa, x, lane * 16);
    gload4(xb, x, 1024 + lane * 16);
    gload4(xc, x, 2048 + (lane & 15) * 16);
    gloadh(xo0, x, (m0 + min(lane, 11)) * 2);
    gloadh(xo1, x, 1152 + (m0 + min(lane, 11)) * 2);
    const unsigned char* b = (const unsigned char*)wq + (size_t)m0 * 8192 + h * 256;
#pragma unroll
    for (int i = 0; i < 3; ++i) gload4nt(wqv[i], b + i * 8192, (3 * rg) * 8192 + ch * 16);
    if (isC) {
      const unsigned char* b2 = (const unsigned char*)wuk + (size_t)(64 * cs + 16 * wv) * 8192 + h * 256;
#pragma unroll
      for (int i = 0; i < 4; ++i) gload4nt(wukv[i], b2 + i * 8192, (4 * rg) * 8192 + ch * 16);
    }
  }
  bar();
  if (isA) {
    if (isC) waitv<7>(); else waitv<3>();
    TIE(xa); TIE(xb); TIE(xc); TIE(xo0); TIE(xo1);
    STAMP(1);
    float sa = 0.f, sb = 0.f, sc = 0.f;
#pragma unroll
    for (int k = 0; k < 4; ++k) { sa += blo(xa[k]) + bhi(xa[k]); sb += blo(xb[k]) + bhi(xb[k]); sc += blo(xc[k]) + bhi(xc[k]); }
    const bool b0 = lane < 8, c1 = lane < 16;
    const float m0 = wave_sum(sa + (b0 ? sb : 0.f)) * (1.0f / DM);
    const float m1 = wave_sum((b0 ? 0.f : sb) + (c1 ? sc : 0.f)) * (1.0f / DM);
    const float mb = b0 ? m0 : m1;
    float va = 0.f, vb = 0.f, vc = 0.f;
#pragma unroll
    for (int k = 0; k < 4; ++k) {
      float d;
      d = blo(xa[k]) - m0; va += d * d; d = bhi(xa[k]) - m0; va += d * d;
      d = blo(xb[k]) - mb; vb += d * d; d = bhi(xb[k]) - mb; vb += d * d;
      d = blo(xc[k]) - m1; vc += d * d; d = bhi(xc[k]) - m1; vc += d * d;
    }
    const float v0 = wave_sum(va + (b0 ? vb : 0.f)) * (1.0f / DM);
    const float v1 = wave_sum((b0 ? 0.f : vb) + (c1 ? vc : 0.f)) * (1.0f / DM);
    const float r0 = 1.0f / sqrtf(v0 + 1e-5f), r1 = 1.0f / sqrtf(v1 + 1e-5f);
    if (lane < 12) *(LAS f32x2*)(L + L_XL + (aw * 12 + lane) * 8) = f32x2{(blo(xo0) - m0) * r0, (blo(xo1) - m1) * r1};
    waitl();
    f32x2 xr[3];
#pragma unroll
    for (int i = 0; i < 3; ++i) xr[i] = *(LAS f32x2*)(L + L_XL + (aw * 12 + 3 * rg + i) * 8);
    float a0[8], a1[8];
#pragma unroll
    for (int k = 0; k < 8; ++k) { a0[k] = 0.f; a1[k] = 0.f; }
    if (isC) waitv<4>(); else waitv<0>();
#pragma unroll
    for (int i = 0; i < 3; ++i) { TIE(wqv[i]); fma8(a0, a1, wqv[i], xr[i][0], xr[i][1]); }
    float t[8];
#pragma unroll
    for (int k = 0; k < 8; ++k) t[k] = tr32(a0[k], a1[k]);
    f32x4 o;
#pragma unroll
    for (int k = 0; k < 4; ++k) o[k] = tr16(t[k], t[k + 4]);
    *(LAS f32x4*)(L + L_QP + (aw * 256 + (lane >> 5) * 128 + ch * 8 + (rg & 1) * 4) * 4) = o;
    sig(prog, wv, 1u, lane);
    STAMP(2);
  }

  if (isC) {
    if (wv == 0) {
      wait4(prog, 0, 1u); wait1(prog, 5, 1u); wait1(prog, 6, 1u);
      f32x4 s = *(LAS f32x4*)(L + L_QP + lane * 16);
#pragma unroll
      for (int w = 1; w < 6; ++w) s += *(LAS f32x4*)(L + L_QP + w * 1024 + lane * 16);
      wait1(prog, 12, 1u);
      publish4(rs, WS_Q0 + (h * 8 + cs) * 1024 + lane * 16, s);
    } else {
      wait1(prog, 12, 1u);
    }
    STAMP(3);
    {
      const f32x4 qs = gather8(rs, WS_Q0 + h * 8192, lane);
      STAMP(16);
      *(LAS f32x4*)(L + L_QV + wv * 1024 + lane * 16) = qs;
      waitl();
      LAS unsigned char* qv = L + L_QV + wv * 1024;
      const f32x4 q00 = *(LAS f32x4*)(qv + (ch * 8) * 4), q01 = *(LAS f32x4*)(qv + (ch * 8 + 4) * 4);
      const f32x4 q10 = *(LAS f32x4*)(qv + (128 + ch * 8) * 4), q11 = *(LAS f32x4*)(qv + (128 + ch * 8 + 4) * 4);
      float d0[4], d1[4];
      waitv<0>();
#pragma unroll
      for (int i = 0; i < 4; ++i) {
        TIE(wukv[i]);
        const v4u w = wukv[i];
        float s0 = blo(w[0]) * q00[0], s1 = blo(w[0]) * q10[0];
        s0 = fmaf(bhi(w[0]), q00[1], s0); s1 = fmaf(bhi(w[0]), q10[1], s1);
        s0 = fmaf(blo(w[1]), q00[2], s0); s1 = fmaf(blo(w[1]), q10[2], s1);
        s0 = fmaf(bhi(w[1]), q00[3], s0); s1 = fmaf(bhi(w[1]), q10[3], s1);
        s0 = fmaf(blo(w[2]), q01[0], s0); s1 = fmaf(blo(w[2]), q11[0], s1);
        s0 = fmaf(bhi(w[2]), q01[1], s0); s1 = fmaf(bhi(w[2]), q11[1], s1);
        s0 = fmaf(blo(w[3]), q01[2], s0); s1 = fmaf(blo(w[3]), q11[2], s1);
        s0 = fmaf(bhi(w[3]), q01[3], s0); s1 = fmaf(bhi(w[3]), q11[3], s1);
        d0[i] = sum_row16(s0); d1[i] = sum_row16(s1);
      }
      constexpr float QS = 0.08838834764831845f * 1.4426950408889634f;
      const bool sel = ch & 1;
      v2u o;
      o.x = sane(cvtpk((sel ? d1[0] : d0[0]) * QS, (sel ? d1[1] : d0[1]) * QS));
      o.y = sane(cvtpk((sel ? d1[2] : d0[2]) * QS, (sel ? d1[3] : d0[3]) * QS));
      const int c = 64 * cs + 16 * wv + 4 * rg;
      const int off = WS_QA + (ch & 1) * 32768 + (c >> 4) * 1024 + (((c >> 3) & 1) * 32 + h) * 16 + (c & 7) * 2;
      if (ch < 2) __builtin_amdgcn_raw_buffer_store_b64(o, rs, off, 0, AUX_SC1);
    }
    sig(prog, wv, 3u, lane);
    STAMP(4);
    v4u q31;
    {
      v4u qf[8];
      const int base = WS_QA + ab * 32768 + lane * 16;
      for (unsigned spin = 0; spin < SPIN_CAP; ++spin) {
        q31 = __builtin_amdgcn_raw_buffer_load_b128(rs, base + 31 * 1024, 0, AUX_SC1);
        if (__all(valid4(q31))) break;
        __builtin_amdgcn_s_sleep(1);
      }
      for (unsigned spin = 0; spin < SPIN_CAP; ++spin) {
        bool ok = true;
#pragma unroll
        for (int i = 0; i < 8; ++i) qf[i] = __builtin_amdgcn_raw_buffer_load_b128(rs, base + (8 * wv + i) * 1024, 0, AUX_SC1);
#pragma unroll
        for (int i = 0; i < 8; ++i) ok = ok && valid4(qf[i]);
        if (__all(ok)) break;
        __builtin_amdgcn_s_sleep(1);
      }
      wait4(prog, 0, 3u);
#pragma unroll
      for (int i = 0; i < 8; ++i)
        if (8 * wv + i < 31) *(LAS v4u*)(L + L_S + (8 * wv + i) * 1024 + lane * 16) = qf[i];
    }
    sig(prog, wv, 4u, lane);
    STAMP(5);
    wait4(prog, 0, 4u);
    wait4(prog, 8, 2u);
    STAMP(6);
    const int r31 = lane & 31, hh = lane >> 5;
    f32x16 sacc;
#pragma unroll
    for (int i = 0; i < 16; ++i) sacc[i] = 0.f;
    {
      const int row = 32 * wv + r31;
      const int sw = ((row & 3) << 2) | ((row >> 2) & 3);
      LAS unsigned char* arow = L + L_C + row * 1024;
      constexpr int W = 6;
      v4u af[32], bf[32];
#pragma unroll
      for (int s = 0; s < W; ++s) {
        const int c64 = 2 * s + hh;
        af[s] = *(LAS v4u*)(arow + (c64 >> 4) * 256 + (((c64 & 15) ^ sw) * 16));
        bf[s] = *(LAS v4u*)(L + L_S + s * 1024 + lane * 16);
      }
      __builtin_amdgcn_sched_barrier(0);
#pragma unroll
      for (int s = 0; s < 32; ++s) {
        if (s + W < 32) {
          const int c64 = 2 * (s + W) + hh;
          af[s + W] = *(LAS v4u*)(arow + (c64 >> 4) * 256 + (((c64 & 15) ^ sw) * 16));
          bf[s + W] = s + W < 31 ? *(LAS v4u*)(L + L_S + (s + W) * 1024 + lane * 16) : q31;
        }
        sacc = __builtin_amdgcn_mfma_f32_32x32x16_bf16(__builtin_bit_cast(bf16x8, af[s]), __builtin_bit_cast(bf16x8, bf[s]), sacc, 0, 0, 0);
        __builtin_amdgcn_sched_barrier(0);
      }
    }
    float mx = sacc[0];
#pragma unroll
    for (int i = 1; i < 16; ++i) mx = fmaxf(mx, sacc[i]);
    mx = xmax32(mx);
    if (lane < 32) *(LAS float*)(L + L_MX + (wv * 32 + lane) * 4) = mx;
    sig(prog, wv, 5u, lane);
    STAMP(17);
    wait4(prog, 0, 5u);
#pragma unroll
    for (int w = 0; w < 4; ++w) mx = fmaxf(mx, *(LAS float*)(L + L_MX + (w * 32 + r31) * 4));
    {
      float ls = 0.f;
      v4u pf[2];
#pragma unroll
      for (int i = 0; i < 8; ++i) {
        const float p0 = __builtin_amdgcn_exp2f(sacc[2 * i] - mx), p1 = __builtin_amdgcn_exp2f(sacc[2 * i + 1] - mx);
        const unsigned pk = cvtpk(p0, p1);
        ls += blo(pk) + bhi(pk);
        pf[i >> 2][i & 3] = pk;
      }
      ls = xsum32(ls);
      *(LAS v4u*)(L + L_P + (wv * 2 + 0) * 1024 + lane * 16) = pf[0];
      *(LAS v4u*)(L + L_P + (wv * 2 + 1) * 1024 + lane * 16) = pf[1];
      if (lane < 32) *(LAS float*)(L + L_LS + (wv * 32 + lane) * 4) = ls;
    }
    sig(prog, wv, 6u, lane);
    wait4(prog, 0, 6u);
    STAMP(7);
    f32x16 oacc[4];
#pragma unroll
    for (int cb = 0; cb < 4; ++cb)
#pragma unroll
      for (int i = 0; i < 16; ++i) oacc[cb][i] = 0.f;
    {
      const int blk = (lane >> 4) & 1, q4 = (lane & 15) >> 2, p4 = lane & 3;
      LAS unsigned char* tb = L + L_C + (4 * hh + q4) * 1024 + wv * 256 + 8 * (p4 & 1);
      v4u pb[8];
#pragma unroll
      for (int ks = 0; ks < 8; ++ks) pb[ks] = *(LAS v4u*)(L + L_P + ks * 1024 + lane * 16);
      constexpr int W = 5;
      s16x4 a[32][2];
#define TRLD(n) { _Pragma("unroll") for (int t = 0; t < 2; ++t) { \
        const int chunk = ((((n) & 3) ^ q4) << 2) | ((2 * blk + (p4 >> 1)) ^ (2 * t + hh)); \
        a[n][t] = __builtin_amdgcn_ds_read_tr16_b64_v4i16((LAS s16x4*)(tb + (16 * ((n) >> 2) + 8 * t) * 1024 + chunk * 16)); } }
#pragma unroll
      for (int n = 0; n < W; ++n) TRLD(n)
      __builtin_amdgcn_sched_barrier(0);
#pragma unroll
      for (int n = 0; n < 32; ++n) {
        if (n + W < 32) TRLD(n + W)
        const bf16x8 A = __builtin_shufflevector(a[n][0], a[n][1], 0, 1, 2, 3, 4, 5, 6, 7);
        oacc[n & 3] = __builtin_amdgcn_mfma_f32_32x32x16_bf16(A, __builtin_bit_cast(bf16x8, pb[n >> 2]), oacc[n & 3], 0, 0, 0);
        __builtin_amdgcn_sched_barrier(0);
      }
#undef TRLD
    }
    STAMP(18);
    {
      float lt = 0.f;
#pragma unroll
      for (int w = 0; w < 4; ++w) lt += *(LAS float*)(L + L_LS + (w * 32 + r31) * 4);
      sig(prog, wv, 7u, lane);
      const float inv = 1.0f / lt;
#pragma unroll
      for (int cb = 0; cb < 4; ++cb) {
#pragma unroll
        for (int g = 0; g < 4; g += 2) {
          unsigned a0 = cvtpk(oacc[cb][4 * g] * inv, oacc[cb][4 * g + 1] * inv), a1 = cvtpk(oacc[cb][4 * g + 2] * inv, oacc[cb][4 * g + 3] * inv);
          unsigned b0 = cvtpk(oacc[cb][4 * g + 4] * inv, oacc[cb][4 * g + 5] * inv), b1 = cvtpk(oacc[cb][4 * g + 6] * inv, oacc[cb][4 * g + 7] * inv);
          auto s0 = __builtin_amdgcn_permlane32_swap(a0, b0, false, false);
          auto s1 = __builtin_amdgcn_permlane32_swap(a1, b1, false, false);
          const v4u o = {s0[0], s1[0], s0[1], s1[1]};
          const int cl = 32 * cb + 8 * (g + hh);
          const int off = WS_PART + ((((r31 * 8 + 2 * wv + (cl >> 6)) * 2 + ab) * 128 + asplit) * 64 + (cl & 63)) * 2;
          __builtin_amdgcn_raw_buffer_store_b128(o, rs, off, 0, AUX_SC1);
        }
      }
      STAMP(8);
      waitv<0>();
      if (lane < 32) {
        const float lse = mx + __builtin_amdgcn_logf(lt);
        __builtin_amdgcn_raw_buffer_store_b32(sanef(lse), rs, WS_LSE + (((lane * 4 + wv) * 2 + ab) * 128 + asplit) * 4, 0, AUX_SC1);
      }
    }
    STAMP(9);
    {
      v4u lv;
      for (unsigned spin = 0; spin < SPIN_CAP; ++spin) {
        lv = __builtin_amdgcn_raw_buffer_load_b128(rs, WS_LSE + (h * 4 + (cs >> 1)) * 1024 + lane * 16, 0, AUX_SC1);
        if (__all(valid4(lv))) break;
        __builtin_amdgcn_s_sleep(1);
      }
      cfence();
      STAMP(10);
      v4u pv[8];
      const int pbase = WS_PART + (((h * 8 + cs) * 2 + (wv >> 1)) * 128 + 64 * (wv & 1)) * 128 + lane * 16;
#pragma unroll
      for (int i = 0; i < 8; ++i) pv[i] = __builtin_amdgcn_raw_buffer_load_b128(rs, pbase + i * 1024, 0, AUX_SC1);
      if (wg == 0 && wv == 3 && lane == 0) {
        u64 e = __hip_atomic_load((u64*)(ws + WS_EPOCH), __ATOMIC_RELAXED, __HIP_MEMORY_SCOPE_AGENT);
        __hip_atomic_store((u64*)(ws + WS_EPOCH), e + 1ull, __ATOMIC_RELAXED, __HIP_MEMORY_SCOPE_AGENT);
      }
      float m = fmaxf(fmaxf(asf(lv.x), asf(lv.y)), fmaxf(asf(lv.z), asf(lv.w)));
      m = xmax16(max_row16(m));
      f32x4 e;
#pragma unroll
      for (int k = 0; k < 4; ++k) e[k] = __builtin_amdgcn_exp2f(asf(lv[k]) - m);
      const float tot = xsum16(sum_row16((e[0] + e[1]) + (e[2] + e[3])));
      const float it = 1.0f / tot;
      wait4(prog, 0, 7u);
      *(LAS f32x4*)(L + L_WG + wv * 1024 + lane * 16) = e * it;
      waitl();
      float acc[8];
#pragma unroll
      for (int k = 0; k < 8; ++k) acc[k] = 0.f;
#pragma unroll
      for (int i = 0; i < 8; ++i) {
        const float w = *(LAS float*)(L + L_WG + wv * 1024 + ((wv >> 1) * 128 + 64 * (wv & 1) + 8 * i + (lane >> 3)) * 4);
#pragma unroll
        for (int k = 0; k < 4; ++k) { acc[2 * k] = fmaf(w, blo(pv[i][k]), acc[2 * k]); acc[2 * k + 1] = fmaf(w, bhi(pv[i][k]), acc[2 * k + 1]); }
      }
      float t[4], u2[2];
#pragma unroll
      for (int k = 0; k < 4; ++k) t[k] = tr32(acc[k], acc[k + 4]);
#pragma unroll
      for (int k = 0; k < 2; ++k) u2[k] = tr16(t[k], t[k + 2]);
      u2[0] += dpp<ROR8>(u2[0]); u2[1] += dpp<ROR8>(u2[1]);
      if ((lane & 8) == 0) *(LAS f32x2*)(L + L_CC + (wv * 64 + (lane & 7) * 8 + (lane >> 5) * 4 + ((lane >> 4) & 1) * 2) * 4) = f32x2{u2[0], u2[1]};
    }
    STAMP(11);
    bar();
    bar();
    if (wv == 0) {
      f32x4 s = *(LAS f32x4*)(L + L_CU + lane * 16);
#pragma unroll
      for (int w = 1; w < 8; ++w) s += *(LAS f32x4*)(L + L_CU + w * 1024 + lane * 16);
      publish4(rs, WS_CX + (h * 8 + cs) * 1024 + lane * 16, s);
      STAMP(19);
      const f32x4 cx = gather8(rs, WS_CX + h * 8192, lane);
      const int d = (lane & 31) * 4, b = lane >> 5;
#pragma unroll
      for (int e = 0; e < 4; ++e) *(LAS float*)(L + L_CTX + ((d + e) * 2 + b) * 4) = cx[e];
      STAMP(20);
    }
    bar();
    bar();
  } else {
    if (isAdm) {
      waitv<0>();
      TIE(ep);
      ep += 0x9E3779B97F4A7C15ull;
      if (lane < NREP) gstore2sc(ep, ws, WS_FLAG + lane * 2048 + wg * 8);
      STAMP(1);
      const unsigned elo = (unsigned)ep, ehi = (unsigned)(ep >> 32);
      for (unsigned spin = 0; spin < SPIN_CAP; ++spin) {
        const v4u a = __builtin_amdgcn_raw_buffer_load_b128(rs, WS_FLAG + (wg & (NREP - 1)) * 2048 + lane * 32, 0, AUX_SC1);
        const v4u b = __builtin_amdgcn_raw_buffer_load_b128(rs, WS_FLAG + (wg & (NREP - 1)) * 2048 + lane * 32 + 16, 0, AUX_SC1);
        const bool ok = a.x == elo && a.y == ehi && a.z == elo && a.w == ehi && b.x == elo && b.y == ehi && b.z == elo && b.w == ehi;
        if (__all(ok)) break;
        __builtin_amdgcn_s_sleep(1);
      }
      waitv<0>();
      sig(prog, 12, 1u, lane);
      STAMP(3);
    }
    if (isDma) {
      const unsigned char* b = (const unsigned char*)ckv + ((size_t)ab * SK + asplit * 128) * 1024;
      const unsigned lb = (unsigned)(uintptr_t)(L + L_C);
#pragma unroll
      for (int i = 0; i < 32; ++i) {
        const int r = 4 * i + (wv - 8);
        const int sw = ((r & 3) << 2) | ((r >> 2) & 3);
        if (i >= 4) waitv<3>();
        dma16(b + r * 1024, (unsigned)(((lane & 48) | ((lane & 15) ^ sw)) * 16), lb + r * 1024);
      }
      if (wv == 8) sig(prog, 13, 1u, lane);
      waitv<0>();
      sig(prog, wv, 2u, lane);
      STAMP(3);
    } else {
      for (;;) {
        unsigned a = *(volatile LAS unsigned*)(prog + 13);
        if (__builtin_amdgcn_readfirstlane(a) >= 1u) break;
        __builtin_amdgcn_s_sleep(4);
      }
      cfence();
    }
    v4u wuvr[2], wor[3], wdr[3]; unsigned wgur[21];
    const int td = tid - 256;
    const int wd = wv - 4;
    const int orp = min(lane / 9, 6), oc9 = lane % 9;
    const int gu = td % 18, gg = min(td / 18, 27);
    const int dg = min(td / 72, 5), oc = td % 72;
    {
      const unsigned char* b = (const unsigned char*)wuv + (size_t)(64 * cs + 8 * wd) * 8192 + h * 256;
      gload4nt(wuvr[0], b, rg * 8192 + ch * 16);
      gload4nt(wuvr[1], b + 4 * 8192, rg * 8192 + ch * 16);
      const unsigned char* wob = (const unsigned char*)wo + (size_t)(h * 128 + 16 * wd) * 1152 + cs * 144;
      waitv<1>();
      gload4nt(wor[0], wob, orp * 1152 + oc9 * 16);
      waitv<1>();
      gload4nt(wor[1], wob, (7 + orp) * 1152 + oc9 * 16);
      waitv<1>();
      gload4nt(wor[2], wob, min(14 + orp, 15) * 1152 + oc9 * 16);
      const unsigned char* p = (const unsigned char*)(gu < 9 ? wgt : wup) + (size_t)gg * 9216 + f0 * 2 + (gu % 9) * 4;
#pragma unroll
      for (int i = 0; i < 20; ++i) {
        if (i == 0) waitv<1>();
        if (i >= 6 && (i % 3) == 0) waitv<4>();
        gload1p(wgur[i], p + (size_t)i * 28 * 9216);
      }
      gload1p(wgur[20], p + (size_t)(gg < 16 ? 560 : 0) * 9216);
      const unsigned char* b3 = (const unsigned char*)wdn + (size_t)f0 * 1152;
      waitv<4>();
      gload4nt(wdr[0], b3, dg * 3 * 1152 + oc * 16);
      waitv<2>();
      gload4nt(wdr[1], b3 + 1152, dg * 3 * 1152 + oc * 16);
      waitv<1>();
      gload4nt(wdr[2], b3 + 2304, dg * 3 * 1152 + oc * 16);
    }
    STAMP(4);
    bar();
    {
      TIE(wuvr[0]); TIE(wuvr[1]);
      float a0[8], a1[8];
#pragma unroll
      for (int k = 0; k < 8; ++k) { a0[k] = 0.f; a1[k] = 0.f; }
#pragma unroll
      for (int i = 0; i < 2; ++i) {
        const int c = 8 * wd + 4 * i + rg;
        const float x0 = *(LAS float*)(L + L_CC + c * 4) + *(LAS float*)(L + L_CC + (64 + c) * 4);
        const float x1 = *(LAS float*)(L + L_CC + (128 + c) * 4) + *(LAS float*)(L + L_CC + (192 + c) * 4);
        fma8(a0, a1, wuvr[i], x0, x1);
      }
      float t[8];
#pragma unroll
      for (int k = 0; k < 8; ++k) t[k] = tr32(a0[k], a1[k]);
      f32x4 o;
#pragma unroll
      for (int k = 0; k < 4; ++k) o[k] = tr16(t[k], t[k + 4]);
      *(LAS f32x4*)(L + L_CU + (wd * 256 + (lane >> 5) * 128 + ch * 8 + (rg & 1) * 4) * 4) = o;
    }
    bar();
    bar();
    {
      float a0[8], a1[8];
#pragma unroll
      for (int k = 0; k < 8; ++k) { a0[k] = 0.f; a1[k] = 0.f; }
#pragma unroll
      for (int i = 0; i < 3; ++i) {
        TIE(wor[i]);
        const int d = 7 * i + orp;
        f32x2 xv = *(LAS f32x2*)(L + L_CTX + (16 * wd + min(d, 15)) * 8);
        if (d > 15) xv = f32x2{0.f, 0.f};
        fma8(a0, a1, wor[i], xv[0], xv[1]);
      }
      TIE8(a0); TIE8(a1);
      if (lane < 63) {
        LAS float* o = (LAS float*)(L + L_OPL + ((wd * 7 + orp) * 148 + oc9 * 8) * 4);
        *(LAS f32x4*)(o) = f32x4{a0[0], a0[1], a0[2], a0[3]};
        *(LAS f32x4*)(o + 4) = f32x4{a0[4], a0[5], a0[6], a0[7]};
        *(LAS f32x4*)(o + 72) = f32x4{a1[0], a1[1], a1[2], a1[3]};
        *(LAS f32x4*)(o + 76) = f32x4{a1[4], a1[5], a1[6], a1[7]};
      }
    }
    bar();
#define OPRED() if (tid < 576) { \
      const int q4 = tid >> 4, sp = tid & 15; \
      f32x4 s = *(LAS f32x4*)(L + L_OPL + (sp * 148 + q4 * 4) * 4); \
      s += *(LAS f32x4*)(L + L_OPL + ((sp + 16) * 148 + q4 * 4) * 4); \
      s += *(LAS f32x4*)(L + L_OPL + ((sp + 32) * 148 + q4 * 4) * 4); \
      if (sp < 8) s += *(LAS f32x4*)(L + L_OPL + ((sp + 48) * 148 + q4 * 4) * 4); \
      _Pragma("unroll") for (int k = 0; k < 4; ++k) s[k] = sum_row16(s[k]); \
      const int o = (q4 >= 18 ? 576 - 72 : 0) + 72 * cs + q4 * 4; \
      if (sp == 0) publish4(rs, WS_OP + ((o >> 4) * 32 + h) * 64 + (o & 15) * 4, s); \
    }
    OPRED()
    STAMP(12);
    bar();
    {
      waitv<3>();
      f32x4 a = {0.f, 0.f, 0.f, 0.f};
#pragma unroll
      for (int i = 0; i < 21; ++i) {
        TIE(wgur[i]);
        const int m = gg + 28 * i;
        f32x2 xv = *(LAS f32x2*)(L + L_XN2 + min(m, 575) * 8);
        if (m > 575) xv = f32x2{0.f, 0.f};
        const float lo = blo(wgur[i]), hi = bhi(wgur[i]);
        a[0] = fmaf(lo, xv[0], a[0]); a[1] = fmaf(lo, xv[1], a[1]); a[2] = fmaf(hi, xv[0], a[2]); a[3] = fmaf(hi, xv[1], a[3]);
      }
      TIE(a);
      if (td < 504) *(LAS f32x4*)(L + L_GU + (gg * 18 + gu) * 16) = a;
    }
    bar();
    bar();
    {
      waitv<0>();
      float a0[8], a1[8];
#pragma unroll
      for (int k = 0; k < 8; ++k) { a0[k] = 0.f; a1[k] = 0.f; }
#pragma unroll
      for (int i = 0; i < 3; ++i) {
        TIE(wdr[i]);
        const f32x2 xv = *(LAS f32x2*)(L + L_ACT + (3 * dg + i) * 8);
        fma8(a0, a1, wdr[i], xv[0], xv[1]);
      }
      TIE8(a0); TIE8(a1);
      if (td < 432) {
        LAS float* o = (LAS float*)(L + L_DPL + (dg * 1152 + oc * 8) * 4);
        *(LAS f32x4*)(o) = f32x4{a0[0], a0[1], a0[2], a0[3]};
        *(LAS f32x4*)(o + 4) = f32x4{a0[4], a0[5], a0[6], a0[7]};
        *(LAS f32x4*)(o + 576) = f32x4{a1[0], a1[1], a1[2], a1[3]};
        *(LAS f32x4*)(o + 580) = f32x4{a1[4], a1[5], a1[6], a1[7]};
      }
    }
    bar();
    if (td < 288) {
      f32x4 s = *(LAS f32x4*)(L + L_DPL + (td * 4) * 4);
#pragma unroll
      for (int g = 1; g < 6; ++g) s += *(LAS f32x4*)(L + L_DPL + (g * 1152 + td * 4) * 4);
      publish4(rs, WS_DP + ((td >> 2) * 256 + wg) * 64 + (td & 3) * 16, s);
    }
    STAMP(14);
    return;
  }
  OPRED()
  STAMP(12);
  f32x4 hown = {0.f, 0.f, 0.f, 0.f};
  if (wv == 1 && owner) {
    const v2u xv = *(const v2u*)(x + own * 16 + (lane & 3) * 4);
    const f32x4 s = own_reduce<2>(rs, WS_OP + own * 2048, lane);
    hown = f32x4{blo(xv.x) + s[0], bhi(xv.x) + s[1], blo(xv.y) + s[2], bhi(xv.y) + s[3]};
    if (lane < 4) publish4(rs, WS_HB + own * 64 + lane * 16, hown);
    STAMP(21);
  }
  if (wv == 0) {
    v4u hv[5];
    for (unsigned spin = 0; spin < SPIN_CAP; ++spin) {
      bool ok = true;
#pragma unroll
      for (int k = 0; k < 4; ++k) hv[k] = __builtin_amdgcn_raw_buffer_load_b128(rs, WS_HB + k * 1024 + lane * 16, 0, AUX_SC1);
      hv[4] = __builtin_amdgcn_raw_buffer_load_b128(rs, WS_HB + 4096 + (lane & 31) * 16, 0, AUX_SC1);
#pragma unroll
      for (int k = 0; k < 5; ++k) ok = ok && valid4(hv[k]);
      if (__all(ok)) break;
      __builtin_amdgcn_s_sleep(1);
    }
    STAMP(13);
    f32x4 f[5];
#pragma unroll
    for (int k = 0; k < 5; ++k) f[k] = __builtin_bit_cast(f32x4, hv[k]);
    const bool m2 = lane < 16, m4 = lane < 32;
    float s[5];
#pragma unroll
    for (int k = 0; k < 5; ++k) s[k] = (f[k][0] + f[k][1]) + (f[k][2] + f[k][3]);
    const float m0 = wave_sum(s[0] + s[1] + (m2 ? s[2] : 0.f)) * (1.0f / DM);
    const float m1 = wave_sum((m2 ? 0.f : s[2]) + s[3] + (m4 ? s[4] : 0.f)) * (1.0f / DM);
    const float mm[5] = {m0, m0, m2 ? m0 : m1, m1, m1};
#pragma unroll
    for (int k = 0; k < 5; ++k) { f[k] -= mm[k]; s[k] = (f[k][0] * f[k][0] + f[k][1] * f[k][1]) + (f[k][2] * f[k][2] + f[k][3] * f[k][3]); }
    const float v0 = wave_sum(s[0] + s[1] + (m2 ? s[2] : 0.f)) * (1.0f / DM);
    const float v1 = wave_sum((m2 ? 0.f : s[2]) + s[3] + (m4 ? s[4] : 0.f)) * (1.0f / DM);
    const float r0 = 1.0f / sqrtf(v0 + 1e-5f), r1 = 1.0f / sqrtf(v1 + 1e-5f);
    const float rr[5] = {r0, r0, m2 ? r0 : r1, r1, r1};
#pragma unroll
    for (int k = 0; k < 5; ++k) {
      const int i4 = 64 * k + lane;
      if (k < 4 || m4) {
        const int b = i4 >= 144 ? 1 : 0, m = (i4 - 144 * b) * 4;
#pragma unroll
        for (int e = 0; e < 4; ++e) *(LAS float*)(L + L_XN2 + ((m + e) * 2 + b) * 4) = f[k][e] * rr[k];
      }
    }
    STAMP(22);
  }
  bar();
  bar();
  if (tid < 72) {
    const int dw = tid >> 3, mat = (tid >> 2) & 1, gq = tid & 3;
    f32x4 s = {0.f, 0.f, 0.f, 0.f};
#pragma unroll
    for (int k = 0; k < 7; ++k) s += *(LAS f32x4*)(L + L_GU + ((7 * gq + k) * 18 + mat * 9 + dw) * 16);
    f32x4 o;
#pragma unroll
    for (int k = 0; k < 4; ++k) {
      float t = s[k]; t += dpp<XOR1>(t); t += dpp<XOR2>(t);
      const float u = dpp<HMIR>(t);
      o[k] = t / (1.0f + __expf(-t)) * u;
    }
    if ((tid & 7) == 0) *(LAS f32x4*)(L + L_ACT + dw * 16) = o;
  }
  bar();
  bar();
  STAMP(14);
  if (wv == 1 && owner) {
    const f32x4 s = own_reduce<16>(rs, WS_DP + own * 16384, lane);
    v2u o;
    o.x = cvtpk(hown[0] + s[0], hown[1] + s[1]);
    o.y = cvtpk(hown[2] + s[2], hown[3] + s[3]);
    if (lane < 4) *(v2u*)(out + own * 16 + lane * 4) = o;
    STAMP(15);
  }
}
}

extern "C" void kernel_launch(void* const* d_in, const int* in_sizes, int n_in,
                              void* d_out, int out_size, void* d_ws, size_t ws_size,
                              hipStream_t stream) {
  using mk::u16;
  (void)hipFuncSetAttribute((const void*)mk::fused, hipFuncAttributeMaxDynamicSharedMemorySize, mk::L_BYTES);
  mk::fused<<<mk::NWG, mk::NT, mk::L_BYTES, stream>>>(
      (const u16*)d_in[0], (const u16*)d_in[1], (const u16*)d_in[2], (const u16*)d_in[3], (const u16*)d_in[4], (const u16*)d_in[5],
      (const u16*)d_in[7], (const u16*)d_in[8], (const u16*)d_in[9], (u16*)d_out, (unsigned char*)d_ws);
}
```

```cpp
#include <hip/hip_runtime.h>
#include <hip/hip_bf16.h>
#include <stdint.h>

#define MK_STAMPS 0
#define DMA_DEPTH 16

#define GLOAD_WQ gload4nt
#define SL0 4
#define SL1 4
#define TAIL_AT 0
#define TAIL_PACE 63
#define PACE() waitv<TAIL_PACE - 1>()
#define NB1 3
#define DMA_D0 2
#define DMA_SW 1u
namespace mk {
constexpr int SK = 16384, DM = 576;
constexpr int NWG = 256, NT = 768;
constexpr int NOWN = 72;
constexpr int NFF = 216;

constexpr int WS_EPOCH = 0;
constexpr int WS_FLAG = 4096, NREP = 8;
constexpr int WS_QA = 65536;
constexpr int WS_Q0 = WS_QA + 65536;
constexpr int WS_T0 = WS_Q0 + 262144;
constexpr int WS_T1 = WS_T0 + 32768;
constexpr int WS_CLR = WS_T1 + NREP * 8192;
constexpr int WS_HB = WS_CLR;
constexpr int WS_CX = WS_HB + 8192;
constexpr int WS_OP = WS_CX + 262144;
constexpr int WS_DP = WS_OP + NOWN * 32 * 64;
constexpr int WS_GX = WS_DP + NOWN * NFF * 64;
constexpr int WS_LSE = WS_GX + NFF * 1024;
constexpr int WS_CLR_END = WS_LSE + 32 * 4 * 256 * 16;
constexpr int CLR_PER_WG = (WS_CLR_END - WS_CLR) / NWG;
static_assert(CLR_PER_WG * NWG == WS_CLR_END - WS_CLR && CLR_PER_WG % 16 == 0 && CLR_PER_WG <= 9 * 1024, "clear split");
constexpr int WS_PART = 4 << 20;
constexpr int WS_STAMP = 24 << 20;
constexpr int WS_BYTES = 32 << 20;

constexpr int L_C = 0;
constexpr int L_S = 131072;
constexpr int L_CTRL = 162816;
constexpr int L_BYTES = 163840;
constexpr int L_XL = L_S;
constexpr int L_QP = L_S + 5120;
constexpr int L_QV = L_S + 13312;
constexpr int L_QO = L_S + 14336;
constexpr int QPITCH = 992;
constexpr int L_P = L_S;
constexpr int L_LS = L_S + 8192;
constexpr int L_MX = L_CTRL + 128;
constexpr int I_UV = 0;
constexpr int I_WO = 16384;
constexpr int I_GU = 34944;
constexpr int I_DN = 88064;
constexpr int L_ST = 145536;
constexpr int T_WG = L_ST;
constexpr int T_CC = L_ST + 1024;
constexpr int T_CX = L_ST + 2048;
constexpr int T_XN = L_ST + 4096;
constexpr int T_ACT = L_ST + 9216;
static_assert(I_GU + 192 * 272 <= I_DN && I_DN + 32 * 1168 <= L_S && L_ST + 16384 <= L_CTRL, "LDS map");

#define LAS __attribute__((address_space(3)))
typedef unsigned v4u __attribute__((ext_vector_type(4)));
typedef unsigned v2u __attribute__((ext_vector_type(2)));
typedef float f32x4 __attribute__((ext_vector_type(4)));
typedef float f32x2 __attribute__((ext_vector_type(2)));
typedef float f32x16 __attribute__((ext_vector_type(16)));
typedef short bf16x8 __attribute__((ext_vector_type(8)));
typedef short s16x4 __attribute__((ext_vector_type(4)));
typedef unsigned short u16;
typedef unsigned long long u64;
#define DI __device__ __forceinline__
constexpr int AUX_SC1 = 16;
constexpr unsigned SENT = 0xFFFFFFFFu;
constexpr unsigned SPIN_CAP = 1u << 16;

DI float asf(unsigned u) { return __builtin_bit_cast(float, u); }
DI unsigned asu(float f) { return __builtin_bit_cast(unsigned, f); }
DI float blo(unsigned u) { return asf(u << 16); }
DI float bhi(unsigned u) { return asf(u & 0xffff0000u); }
DI unsigned cvtpk(float lo, float hi) { unsigned r; asm("v_cvt_pk_bf16_f32 %0, %1, %2" : "=v"(r) : "v"(lo), "v"(hi)); return r; }
DI unsigned sane(unsigned u) { return u == SENT ? 0x7FC07FC0u : u; }
DI unsigned sanef(float f) { unsigned u = asu(f); return u == SENT ? 0x7FC00000u : u; }
template <int CTRL> DI float dpp(float x) { return asf((unsigned)__builtin_amdgcn_mov_dpp((int)asu(x), CTRL, 0xf, 0xf, true)); }
constexpr int XOR1 = 0xB1, XOR2 = 0x4E, HMIR = 0x141, MIR = 0x140, ROR4 = 0x124, ROR8 = 0x128;
DI float sum_row16(float x) { x += dpp<XOR1>(x); x += dpp<XOR2>(x); x += dpp<HMIR>(x); x += dpp<MIR>(x); return x; }
DI float max_row16(float x) { x = fmaxf(x, dpp<XOR1>(x)); x = fmaxf(x, dpp<XOR2>(x)); x = fmaxf(x, dpp<HMIR>(x)); x = fmaxf(x, dpp<MIR>(x)); return x; }
DI float xsum16(float x) { auto s = __builtin_amdgcn_permlane16_swap(asu(x), asu(x), false, false); return asf(s[0]) + asf(s[1]); }
DI float xsum32(float x) { auto s = __builtin_amdgcn_permlane32_swap(asu(x), asu(x), false, false); return asf(s[0]) + asf(s[1]); }
DI float xmax16(float x) { auto s = __builtin_amdgcn_permlane16_swap(asu(x), asu(x), false, false); return fmaxf(asf(s[0]), asf(s[1])); }
DI float xmax32(float x) { auto s = __builtin_amdgcn_permlane32_swap(asu(x), asu(x), false, false); return fmaxf(asf(s[0]), asf(s[1])); }
DI float tr32(float a, float b) { auto s = __builtin_amdgcn_permlane32_swap(asu(a), asu(b), false, false); return asf(s[0]) + asf(s[1]); }
DI float tr16(float a, float b) { auto s = __builtin_amdgcn_permlane16_swap(asu(a), asu(b), false, false); return asf(s[0]) + asf(s[1]); }
template <int CTRL> DI unsigned dppu(unsigned x) { return (unsigned)__builtin_amdgcn_mov_dpp((int)x, CTRL, 0xf, 0xf, true); }
DI unsigned xor4(const v4u& a) { return (a.x ^ a.y) ^ (a.z ^ a.w); }
DI unsigned add4(const v4u& a) { return (a.x + a.y) + (a.z + a.w); }
DI unsigned wave_xor(unsigned x) {
  x ^= dppu<XOR1>(x); x ^= dppu<XOR2>(x); x ^= dppu<HMIR>(x); x ^= dppu<MIR>(x);
  auto s = __builtin_amdgcn_permlane16_swap(x, x, false, false); x = s[0] ^ s[1];
  auto t = __builtin_amdgcn_permlane32_swap(x, x, false, false); return t[0] ^ t[1];
}
DI unsigned wave_add(unsigned x) {
  x += dppu<XOR1>(x); x += dppu<XOR2>(x); x += dppu<HMIR>(x); x += dppu<MIR>(x);
  auto s = __builtin_amdgcn_permlane16_swap(x, x, false, false); x = s[0] + s[1];
  auto t = __builtin_amdgcn_permlane32_swap(x, x, false, false); return t[0] + t[1];
}
template <bool ADD> DI unsigned tred8(const unsigned (&v)[8], int lane) {
  const bool b2 = lane & 4, b1 = lane & 2, b0 = lane & 1;
  unsigned a[4], b[2];
#pragma unroll
  for (int k = 0; k < 4; ++k) { const unsigned keep = b2 ? v[k + 4] : v[k], send = b2 ? v[k] : v[k + 4], r = dppu<HMIR>(send); a[k] = ADD ? keep + r : keep ^ r; }
#pragma unroll
  for (int k = 0; k < 2; ++k) { const unsigned keep = b1 ? a[k + 2] : a[k], send = b1 ? a[k] : a[k + 2], r = dppu<XOR2>(send); b[k] = ADD ? keep + r : keep ^ r; }
  const unsigned keep = b0 ? b[1] : b[0], send = b0 ? b[0] : b[1], r = dppu<XOR1>(send);
  return ADD ? keep + r : keep ^ r;
}
DI float wave_sum(float x) { return xsum32(xsum16(sum_row16(x))); }

template <int N> DI void waitv() { asm volatile("s_waitcnt vmcnt(%0)" ::"i"(N) : "memory"); }
DI void waitl() { asm volatile("s_waitcnt lgkmcnt(0)" ::: "memory"); }
DI void cfence() { asm volatile("" ::: "memory"); }
DI void bar() { asm volatile("s_waitcnt lgkmcnt(0)\n\ts_barrier" ::: "memory"); }
#define TIE(x) asm volatile("" : "+v"(x))
#define TIE8(a) asm volatile("" : "+v"(a[0]), "+v"(a[1]), "+v"(a[2]), "+v"(a[3]), "+v"(a[4]), "+v"(a[5]), "+v"(a[6]), "+v"(a[7]))

DI void gload4(v4u& d, const void* sb, unsigned vo) { asm volatile("global_load_dwordx4 %0, %1, %2" : "=v"(d) : "v"(vo), "s"(sb) : "memory"); }
DI void gload4nt(v4u& d, const void* sb, unsigned vo) { asm volatile("global_load_dwordx4 %0, %1, %2 nt" : "=v"(d) : "v"(vo), "s"(sb) : "memory"); }
DI void gload1p(unsigned& d, const void* p) { asm volatile("global_load_dword %0, %1, off nt" : "=v"(d) : "v"(p) : "memory"); }
DI void gloadh(unsigned& d, const void* sb, unsigned vo) { asm volatile("global_load_ushort %0, %1, %2" : "=v"(d) : "v"(vo), "s"(sb) : "memory"); }
DI void gload2sc(u64& d, const void* sb, unsigned vo) { asm volatile("global_load_dwordx2 %0, %1, %2 sc1" : "=v"(d) : "v"(vo), "s"(sb) : "memory"); }
DI void gstore4sc(const v4u& d, void* sb, unsigned vo) { asm volatile("global_store_dwordx4 %0, %1, %2 sc1\n\ts_nop 1" ::"v"(vo), "v"(d), "s"(sb) : "memory"); }
DI void gstore2sc(const u64& d, void* sb, unsigned vo) { asm volatile("global_store_dwordx2 %0, %1, %2 sc1\n\ts_nop 1" ::"v"(vo), "v"(d), "s"(sb) : "memory"); }
DI void dma16(const void* sb, unsigned vo, unsigned ldsaddr) {
  unsigned keep;
  asm volatile("s_mov_b32 %0, m0\n\ts_mov_b32 m0, %3\n\ts_nop 0\n\tglobal_load_lds_dwordx4 %1, %2 nt\n\ts_mov_b32 m0, %0"
               : "=&s"(keep) : "v"(vo), "s"(sb), "s"(ldsaddr) : "memory");
}

#define STAMP(k) do { } while (0)
#define STAMP_FLUSH() do { } while (0)
#define STAMP_DECL

DI void sig(LAS unsigned* prog, int idx, unsigned v, int lane) {
  waitl();
  if (lane == 0) *(volatile LAS unsigned*)(prog + idx) = v;
  cfence();
}
DI void wait4(LAS unsigned* prog, int first, unsigned v) {
  for (;;) {
    v4u a = *(volatile LAS v4u*)(prog + first);
    unsigned mn = min(min(a.x, a.y), min(a.z, a.w));
    if (__builtin_amdgcn_readfirstlane(mn) >= v) break;
  }
  cfence();
}
DI void wait1(LAS unsigned* prog, int idx, unsigned v) {
  for (;;) {
    unsigned a = *(volatile LAS unsigned*)(prog + idx);
    if (__builtin_amdgcn_readfirstlane(a) >= v) break;
  }
  cfence();
}
DI unsigned max4(const v4u& a) { return max(max(a.x, a.y), max(a.z, a.w)); }

DI void fma8(float (&a0)[8], float (&a1)[8], const v4u& w, float x0, float x1) {
#pragma unroll
  for (int k = 0; k < 4; ++k) {
    const float lo = blo(w[k]), hi = bhi(w[k]);
    a0[2 * k] = fmaf(lo, x0, a0[2 * k]); a1[2 * k] = fmaf(lo, x1, a1[2 * k]);
    a0[2 * k + 1] = fmaf(hi, x0, a0[2 * k + 1]); a1[2 * k + 1] = fmaf(hi, x1, a1[2 * k + 1]);
  }
}

constexpr int OOB = 0x70000000;
template <int N> DI void poll_recs(v4u (&pv)[N], __amdgpu_buffer_rsrc_t rs, int base, bool hiHalf = false) {
  for (unsigned spin = 0; spin < SPIN_CAP; ++spin) {
#pragma unroll
    for (int i = 0; i < N; ++i) pv[i] = __builtin_amdgcn_raw_buffer_load_b128(rs, (N == 14 && i == 13 && hiHalf) ? OOB : base + i * 1024, 0, AUX_SC1);
    cfence();
    unsigned m = max4(pv[0]);
#pragma unroll
    for (int i = 1; i < N; ++i) m = max(m, max4(pv[i]));
    if (__all(m != SENT)) break;
  }
}
template <int N> DI f32x4 own_reduce(__amdgpu_buffer_rsrc_t rs, int base, int lane) {
  v4u pv[N];
  poll_recs<N>(pv, rs, base + lane * 16, lane >= 32);
  f32x4 s = __builtin_bit_cast(f32x4, pv[0]);
#pragma unroll
  for (int i = 1; i < N; ++i) s += __builtin_bit_cast(f32x4, pv[i]);
#pragma unroll
  for (int k = 0; k < 4; ++k) { float t = s[k]; t += dpp<ROR8>(t); t += dpp<ROR4>(t); s[k] = xsum32(xsum16(t)); }
  return s;
}
DI f32x4 gather8(__amdgpu_buffer_rsrc_t rs, int base, int lane) {
  v4u pv[8];
  poll_recs<8>(pv, rs, base + lane * 16);
  f32x4 s = __builtin_bit_cast(f32x4, pv[0]);
#pragma unroll
  for (int i = 1; i < 8; ++i) s += __builtin_bit_cast(f32x4, pv[i]);
  return s;
}
DI void publish4(__amdgpu_buffer_rsrc_t rs, int off, const f32x4& s) {
  const v4u o = {sanef(s[0]), sanef(s[1]), sanef(s[2]), sanef(s[3])};
  __builtin_amdgcn_raw_buffer_store_b128(o, rs, off, 0, AUX_SC1);
}
DI void split4(const f32x4& v, v2u& hi, v2u& lo) {
  hi.x = cvtpk(v[0], v[1]); hi.y = cvtpk(v[2], v[3]);
  lo.x = cvtpk(v[0] - blo(hi.x), v[1] - bhi(hi.x)); lo.y = cvtpk(v[2] - blo(hi.y), v[3] - bhi(hi.y));
}
DI bf16x8 wfrag(LAS unsigned char* p, int pitch) {
  const s16x4 a = __builtin_amdgcn_ds_read_tr16_b64_v4i16((LAS s16x4*)p);
  const s16x4 b = __builtin_amdgcn_ds_read_tr16_b64_v4i16((LAS s16x4*)(p + 4 * pitch));
  return __builtin_shufflevector(a, b, 0, 1, 2, 3, 4, 5, 6, 7);
}
DI f32x4 mfma16(const bf16x8& a, const v4u& b, const f32x4& c) {
  return __builtin_amdgcn_mfma_f32_16x16x32_bf16(a, __builtin_bit_cast(bf16x8, b), c, 0, 0, 0);
}
DI f32x4 hilo(const f32x4& d) { f32x4 r; _Pragma("unroll") for (int k = 0; k < 4; ++k) r[k] = d[k] + dpp<XOR2>(d[k]); return r; }

__global__ void __launch_bounds__(NT, 3)
fused(const u16* __restrict__ x, const u16* __restrict__ ckv, const u16* __restrict__ wq, const u16* __restrict__ wuk,
      const u16* __restrict__ wuv, const u16* __restrict__ wo, const u16* __restrict__ wgt, const u16* __restrict__ wup,
      const u16* __restrict__ wdn, u16* __restrict__ out, unsigned char* __restrict__ ws) {
  extern __shared__ __attribute__((aligned(16))) unsigned char smem[];
  LAS unsigned char* L = (LAS unsigned char*)smem;
  LAS unsigned* prog = (LAS unsigned*)(L + L_CTRL);
  const int tid = threadIdx.x, lane = tid & 63, wv = __builtin_amdgcn_readfirstlane(tid >> 6);
  const int wg = blockIdx.x, xcd = wg & 7, jj = wg >> 3;
  const int h = xcd * 4 + (jj >> 3), cs = jj & 7;
  const int ab = wg >> 7, asplit = wg & 127;
  const bool ffn = jj < 27;
  const int cb = xcd * 9 + min(jj / 3, 8), kt = jj % 3, fi = cb * 3 + kt;
  const int nd = kt < 2 ? 24 : 16;
  const bool owner = jj >= 27 && (jj - 27) * 8 + xcd < 36;
  const int own = ((jj - 27) * 8 + xcd) * 2 + (wv & 1);
  const int rg = lane >> 4, ch = lane & 15;
  const __amdgpu_buffer_rsrc_t rs = __builtin_amdgcn_make_buffer_rsrc((void*)ws, 0, WS_BYTES, 0x00020000);
  const bool isC = wv < 4, isAdm = wv == 4, isDma = wv >= 8;
  STAMP_DECL;
  STAMP(0);
  if (tid < 32) prog[tid] = 0u;
  bar();

  u64 ep = 0;
  v4u xa, xb, xc, wqv[5], wukv[4]; unsigned xo0, xo1;
  if (isAdm) {
    gload2sc(ep, ws, WS_EPOCH);
  } else if (isC) {
    const int m0 = 72 * cs + 18 * wv;
    gload2sc(ep, ws, WS_EPOCH);
    gload4(xa, x, lane * 16);
    gload4(xb, x, 1024 + lane * 16);
    gload4(xc, x, 2048 + (lane & 15) * 16);
    gloadh(xo0, x, (m0 + min(lane, 17)) * 2);
    gloadh(xo1, x, 1152 + (m0 + min(lane, 17)) * 2);
    const unsigned char* b = (const unsigned char*)wq + (size_t)m0 * 8192 + h * 256;
#pragma unroll
    for (int i = 0; i < 4; ++i) gload4nt(wqv[i], b + i * 4 * 8192, rg * 8192 + ch * 16);
    gload4nt(wqv[4], b, (16 + (rg & 1)) * 8192 + ch * 16);
    const unsigned char* b2 = (const unsigned char*)wuk + (size_t)(64 * cs + 16 * wv) * 8192 + h * 256;
#pragma unroll
    for (int i = 0; i < 4; ++i) gload4nt(wukv[i], b2 + i * 8192, (4 * rg) * 8192 + ch * 16);
  }
  if (isC) {
    waitv<9>();
    TIE(xa); TIE(xb); TIE(xc); TIE(xo0); TIE(xo1);
    STAMP(1);
    float sa = 0.f, sb = 0.f, sc = 0.f;
#pragma unroll
    for (int k = 0; k < 4; ++k) { sa += blo(xa[k]) + bhi(xa[k]); sb += blo(xb[k]) + bhi(xb[k]); sc += blo(xc[k]) + bhi(xc[k]); }
    const bool b0 = lane < 8, c1 = lane < 16;
    const float m0 = wave_sum(sa + (b0 ? sb : 0.f)) * (1.0f / DM);
    const float m1 = wave_sum((b0 ? 0.f : sb) + (c1 ? sc : 0.f)) * (1.0f / DM);
    const float mb = b0 ? m0 : m1;
    float va = 0.f, vb = 0.f, vc = 0.f;
#pragma unroll
    for (int k = 0; k < 4; ++k) {
      float d;
      d = blo(xa[k]) - m0; va += d * d; d = bhi(xa[k]) - m0; va += d * d;
      d = blo(xb[k]) - mb; vb += d * d; d = bhi(xb[k]) - mb; vb += d * d;
      d = blo(xc[k]) - m1; vc += d * d; d = bhi(xc[k]) - m1; vc += d * d;
    }
    const float v0 = wave_sum(va + (b0 ? vb : 0.f)) * (1.0f / DM);
    const float v1 = wave_sum((b0 ? 0.f : vb) + (c1 ? vc : 0.f)) * (1.0f / DM);
    const float r0 = __builtin_amdgcn_rsqf(v0 + 1e-5f), r1 = __builtin_amdgcn_rsqf(v1 + 1e-5f);
    if (lane < 20) *(LAS f32x2*)(L + L_XL + (wv * 20 + lane) * 8) = lane < 18 ? f32x2{(blo(xo0) - m0) * r0, (blo(xo1) - m1) * r1} : f32x2{0.f, 0.f};
    waitl();
    f32x2 xr[5];
#pragma unroll
    for (int i = 0; i < 5; ++i) xr[i] = *(LAS f32x2*)(L + L_XL + (wv * 20 + 4 * i + rg) * 8);
    float a0[8], a1[8];
#pragma unroll
    for (int k = 0; k < 8; ++k) { a0[k] = 0.f; a1[k] = 0.f; }
    waitv<4>();
#pragma unroll
    for (int i = 0; i < 5; ++i) { TIE(wqv[i]); fma8(a0, a1, wqv[i], xr[i][0], xr[i][1]); }
    float t[8];
#pragma unroll
    for (int k = 0; k < 8; ++k) t[k] = tr32(a0[k], a1[k]);
    f32x4 o;
#pragma unroll
    for (int k = 0; k < 4; ++k) o[k] = tr16(t[k], t[k + 4]);
    *(LAS f32x4*)(L + L_QP + (wv * 256 + (lane >> 5) * 128 + ch * 8 + (rg & 1) * 4) * 4) = o;
    sig(prog, wv, 1u, lane);
    STAMP(2);
  }

  if (!isC) {
    if (isAdm) {
      const v4u sv = {SENT, SENT, SENT, SENT};
#pragma unroll
      for (int i = 0; i < 9; ++i) gstore4sc(sv, ws, WS_CLR + wg * CLR_PER_WG + min(i * 1024 + lane * 16, CLR_PER_WG - 16));
      waitv<0>();
      TIE(ep);
      ep += 0x9E3779B97F4A7C15ull;
      const unsigned elo = (unsigned)ep, ehi = (unsigned)(ep >> 32);
      const v4u ev = {elo, ehi, elo, ehi};
      if (lane < NREP) gstore4sc(ev, ws, WS_FLAG + lane * 4096 + wg * 16);
      STAMP(1);
      for (unsigned spin = 0; spin < SPIN_CAP; ++spin) {
        __builtin_amdgcn_s_sleep(16);
        v4u a[4];
#pragma unroll
        for (int i = 0; i < 4; ++i) a[i] = __builtin_amdgcn_raw_buffer_load_b128(rs, WS_FLAG + (wg & (NREP - 1)) * 4096 + i * 1024 + lane * 16, 0, AUX_SC1);
        cfence();
        unsigned d = 0;
#pragma unroll
        for (int i = 0; i < 4; ++i) d |= (a[i].x ^ elo) | (a[i].y ^ ehi) | (a[i].z ^ elo) | (a[i].w ^ ehi);
        TIE(d);
        if (__all(d == 0u)) break;
      }
      sig(prog, 12, 1u, lane);
      STAMP(3);
    }
    if (isDma) {
      __builtin_amdgcn_s_sleep(8);
      const unsigned char* b = (const unsigned char*)ckv + ((size_t)ab * SK + asplit * 128) * 1024;
      const unsigned lb = (unsigned)(uintptr_t)(L + L_C);
      bool deep = false;
#pragma unroll
      for (int i = 0; i < 32; ++i) {
        const int r = 4 * i + (wv - 8);
        const int sw = ((r & 3) << 2) | ((r >> 2) & 3);
        if (i >= DMA_D0) {
          if (!deep) deep = __builtin_amdgcn_readfirstlane(*(volatile LAS unsigned*)(prog + 14)) >= DMA_SW;
          if (!deep) waitv<DMA_D0 - 1>(); else if (i >= DMA_DEPTH) waitv<DMA_DEPTH - 1>();
        }
        dma16(b + r * 1024, (unsigned)(((lane & 48) | ((lane & 15) ^ sw)) * 16), lb + r * 1024);
      }
      waitv<0>();
      sig(prog, wv, 2u, lane);
      STAMP(3);
    }
    for (;;) {
      v4u a = *(volatile LAS v4u*)(prog);
      if (__builtin_amdgcn_readfirstlane(min(min(a.x, a.y), min(a.z, a.w))) >= 4u) break;
      __builtin_amdgcn_s_sleep(2);
    }
    cfence();
    v4u wuvr[2], wor[3], wdr[4], wgur[6];
    const int td = tid - 256, wd = wv - 4;
    const int orp = min(lane / 9, 6), oc9 = lane % 9;
    const int gr = (wd >> 1) * 48 + (lane >> 3);
    const int dg = min(td / 72, 6), oc = td % 72;
    {
      const unsigned char* b = (const unsigned char*)wuv + (size_t)(64 * cs + 8 * wd) * 8192 + h * 256;
      gload4nt(wuvr[0], b, rg * 8192 + ch * 16);
      PACE();
      gload4nt(wuvr[1], b + 4 * 8192, rg * 8192 + ch * 16);
      const unsigned char* wob = (const unsigned char*)wo + (size_t)(h * 128 + 16 * wd) * 1152 + cs * 144;
      PACE();
      gload4nt(wor[0], wob, orp * 1152 + oc9 * 16);
      PACE();
      gload4nt(wor[1], wob, (7 + orp) * 1152 + oc9 * 16);
      PACE();
      gload4nt(wor[2], wob, min(14 + orp, 15) * 1152 + oc9 * 16);
    }
    const unsigned char* gup = (const unsigned char*)((wd & 1) ? wup : wgt) + (size_t)(192 * kt) * 9216 + cb * 128;
    if (ffn) {
#pragma unroll
      for (int i = 0; i < NB1; ++i) gload4nt(wgur[i], gup + (size_t)i * 8 * 9216, gr * 9216 + (lane & 7) * 16);
      waitv<NB1>();
    } else {
      waitv<0>();
    }
    STAMP(4);
    for (;;) {
      v4u a = *(volatile LAS v4u*)(prog);
      if (__builtin_amdgcn_readfirstlane(min(min(a.x, a.y), min(a.z, a.w))) >= 7u) break;
      __builtin_amdgcn_s_sleep(2);
    }
    cfence();
    TIE(wuvr[0]); TIE(wuvr[1]); TIE(wor[0]); TIE(wor[1]); TIE(wor[2]);
    *(LAS v4u*)(L + I_UV + (8 * wd + rg) * 256 + ch * 16) = wuvr[0];
    *(LAS v4u*)(L + I_UV + (8 * wd + 4 + rg) * 256 + ch * 16) = wuvr[1];
    if (lane < 63) {
      *(LAS v4u*)(L + I_WO + (16 * wd + orp) * 144 + oc9 * 16) = wor[0];
      *(LAS v4u*)(L + I_WO + (16 * wd + 7 + orp) * 144 + oc9 * 16) = wor[1];
      if (orp < 2) *(LAS v4u*)(L + I_WO + (16 * wd + 14 + orp) * 144 + oc9 * 16) = wor[2];
    }
    sig(prog, wv, 3u, lane);
    if (ffn) {
      for (;;) {
        unsigned a = *(volatile LAS unsigned*)(prog + 13);
        if (__builtin_amdgcn_readfirstlane(a) >= 1u) break;
        __builtin_amdgcn_s_sleep(2);
      }
      cfence();
#pragma unroll
      for (int i = NB1; i < 6; ++i) gload4nt(wgur[i], gup + (size_t)i * 8 * 9216, gr * 9216 + (lane & 7) * 16);
      const unsigned char* b3 = (const unsigned char*)wdn + (size_t)(cb * 64 + kt * 24) * 1152;
#pragma unroll
      for (int i = 0; i < 4; ++i) gload4nt(wdr[i], b3, min(dg + 7 * i, nd - 1) * 1152 + oc * 16);
      waitv<4>();
#pragma unroll
      for (int i = 0; i < 6; ++i) { TIE(wgur[i]); *(LAS v4u*)(L + I_GU + (gr + 8 * i) * 272 + (wd & 1) * 128 + (lane & 7) * 16) = wgur[i]; }
      waitv<0>();
      if (td < 504) {
#pragma unroll
        for (int i = 0; i < 4; ++i) { TIE(wdr[i]); *(LAS v4u*)(L + I_DN + (dg + 7 * i) * 1168 + oc * 16) = dg + 7 * i < nd ? wdr[i] : v4u{0u, 0u, 0u, 0u}; }
        if (dg < 4) *(LAS v4u*)(L + I_DN + (28 + dg) * 1168 + oc * 16) = v4u{0u, 0u, 0u, 0u};
      }
      sig(prog, wv, 4u, lane);
    }
    STAMP(6);
    STAMP_FLUSH();
    return;
  }

  TIE(ep);
  const unsigned elo = __builtin_amdgcn_readfirstlane((unsigned)ep), ehi = __builtin_amdgcn_readfirstlane((unsigned)(ep >> 32));
  const unsigned tag = elo ^ 0x5bd1e995u, saltA = ehi + elo * 0x85EBCA6Bu, saltB = (elo ^ ehi) * 0xC2B2AE35u + 0x27D4EB2Fu;
  if (wv == 0) {
    wait4(prog, 0, 1u);
    f32x4 s = *(LAS f32x4*)(L + L_QP + lane * 16);
#pragma unroll
    for (int w = 1; w < 4; ++w) s += *(LAS f32x4*)(L + L_QP + w * 1024 + lane * 16);
    {
      const v4u su = __builtin_bit_cast(v4u, s);
      __builtin_amdgcn_raw_buffer_store_b128(su, rs, WS_Q0 + (h * 8 + cs) * 1024 + lane * 16, 0, AUX_SC1);
      const v4u tr = {tag, wave_xor(xor4(su)) ^ saltA, wave_add(add4(su)) + saltB, tag};
      __builtin_amdgcn_raw_buffer_store_b128(tr, rs, lane == 0 ? WS_T0 + (h * 8 + cs) * 128 : OOB, 0, AUX_SC1);
    }
    STAMP(3);
    v4u pv[8];
    for (unsigned spin = 0; spin < SPIN_CAP; ++spin) {
      const v4u tr = __builtin_amdgcn_raw_buffer_load_b128(rs, WS_T0 + (h * 8 + (lane & 7)) * 128, 0, AUX_SC1);
#pragma unroll
      for (int i = 0; i < 8; ++i) pv[i] = __builtin_amdgcn_raw_buffer_load_b128(rs, WS_Q0 + (h * 8 + i) * 1024 + lane * 16, 0, AUX_SC1);
      cfence();
      if (__all(tr.x == tag && tr.w == tag)) {
        unsigned vx[8], va[8];
#pragma unroll
        for (int i = 0; i < 8; ++i) { vx[i] = xor4(pv[i]); va[i] = add4(pv[i]); }
        unsigned cx = tred8<false>(vx, lane), ca = tred8<true>(va, lane);
        cx ^= dppu<ROR8>(cx); ca += dppu<ROR8>(ca);
        { auto t = __builtin_amdgcn_permlane16_swap(cx, cx, false, false); cx = t[0] ^ t[1]; }
        { auto t = __builtin_amdgcn_permlane16_swap(ca, ca, false, false); ca = t[0] + t[1]; }
        { auto t = __builtin_amdgcn_permlane32_swap(cx, cx, false, false); cx = t[0] ^ t[1]; }
        { auto t = __builtin_amdgcn_permlane32_swap(ca, ca, false, false); ca = t[0] + t[1]; }
        if (__all((cx ^ saltA) == tr.y && ca + saltB == tr.z)) break;
      }
      __builtin_amdgcn_s_sleep(SL0);
    }
    f32x4 qs = __builtin_bit_cast(f32x4, pv[0]);
#pragma unroll
    for (int i = 1; i < 8; ++i) qs += __builtin_bit_cast(f32x4, pv[i]);
    *(LAS f32x4*)(L + L_QV + lane * 16) = qs;
    sig(prog, 14, 1u, lane);
    STAMP(16);
  } else {
    wait1(prog, 14, 1u);
  }
  {
    LAS unsigned char* qv = L + L_QV;
    const f32x4 q00 = *(LAS f32x4*)(qv + (ch * 8) * 4), q01 = *(LAS f32x4*)(qv + (ch * 8 + 4) * 4);
    const f32x4 q10 = *(LAS f32x4*)(qv + (128 + ch * 8) * 4), q11 = *(LAS f32x4*)(qv + (128 + ch * 8 + 4) * 4);
    float d0[4], d1[4];
    waitv<0>();
#pragma unroll
    for (int i = 0; i < 4; ++i) {
      TIE(wukv[i]);
      const v4u w = wukv[i];
      float s0 = blo(w[0]) * q00[0], s1 = blo(w[0]) * q10[0];
      s0 = fmaf(bhi(w[0]), q00[1], s0); s1 = fmaf(bhi(w[0]), q10[1], s1);
      s0 = fmaf(blo(w[1]), q00[2], s0); s1 = fmaf(blo(w[1]), q10[2], s1);
      s0 = fmaf(bhi(w[1]), q00[3], s0); s1 = fmaf(bhi(w[1]), q10[3], s1);
      s0 = fmaf(blo(w[2]), q01[0], s0); s1 = fmaf(blo(w[2]), q11[0], s1);
      s0 = fmaf(bhi(w[2]), q01[1], s0); s1 = fmaf(bhi(w[2]), q11[1], s1);
      s0 = fmaf(blo(w[3]), q01[2], s0); s1 = fmaf(blo(w[3]), q11[2], s1);
      s0 = fmaf(bhi(w[3]), q01[3], s0); s1 = fmaf(bhi(w[3]), q11[3], s1);
      d0[i] = sum_row16(s0); d1[i] = sum_row16(s1);
    }
    constexpr float QS = 0.08838834764831845f * 1.4426950408889634f;
    const bool sel = ch & 1;
    v2u o;
    o.x = cvtpk((sel ? d1[0] : d0[0]) * QS, (sel ? d1[1] : d0[1]) * QS);
    o.y = cvtpk((sel ? d1[2] : d0[2]) * QS, (sel ? d1[3] : d0[3]) * QS);
    if (ch < 2) *(LAS v2u*)(L + L_QO + (ch & 1) * 128 + (16 * wv + 4 * rg) * 2) = o;
  }
  sig(prog, wv, 3u, lane);
  if (wv == 0) {
    wait4(prog, 0, 3u);
    const v4u o = *(LAS v4u*)(L + L_QO + (lane & 15) * 16);
    __builtin_amdgcn_raw_buffer_store_b128(o, rs, lane < 16 ? WS_QA + (lane >> 3) * 32768 + h * 1024 + cs * 128 + (lane & 7) * 16 : OOB, 0, AUX_SC1);
    unsigned cx = xor4(o), ca = add4(o);
    cx ^= dppu<XOR1>(cx); cx ^= dppu<XOR2>(cx); cx ^= dppu<HMIR>(cx);
    ca += dppu<XOR1>(ca); ca += dppu<XOR2>(ca); ca += dppu<HMIR>(ca);
    const v4u tr = {tag, cx ^ saltA, ca + saltB, tag};
    __builtin_amdgcn_raw_buffer_store_b128(tr, rs, (lane & 7) < 2 ? WS_T1 + ((lane >> 4) + 4 * (lane & 1)) * 8192 + ((lane >> 3) & 1) * 4096 + (h * 8 + cs) * 16 : OOB, 0, AUX_SC1);
    STAMP(4);
    sig(prog, 14, 2u, lane);
  } else {
    wait1(prog, 14, 2u);
  }
  STAMP(24);
  wait4(prog, 8, 2u);
  STAMP(6);
  {
    const int base = WS_QA + ab * 32768;
    v4u qf[8];
    for (unsigned spin = 0; spin < SPIN_CAP; ++spin) {
      const v4u tr = __builtin_amdgcn_raw_buffer_load_b128(rs, WS_T1 + (wg & (NREP - 1)) * 8192 + ab * 4096 + ((8 * wv + (lane & 7)) * 8 + (lane >> 3)) * 16, 0, AUX_SC1);
#pragma unroll
      for (int i = 0; i < 8; ++i) qf[i] = __builtin_amdgcn_raw_buffer_load_b128(rs, base + (8 * wv + i) * 1024 + lane * 16, 0, AUX_SC1);
      cfence();
      if (__all(tr.x == tag && tr.w == tag)) {
        unsigned vx[8], va[8];
#pragma unroll
        for (int i = 0; i < 8; ++i) { vx[i] = xor4(qf[i]); va[i] = add4(qf[i]); }
        const unsigned cx = tred8<false>(vx, lane), ca = tred8<true>(va, lane);
        if (__all((cx ^ saltA) == tr.y && ca + saltB == tr.z)) break;
      }
      __builtin_amdgcn_s_sleep(SL1);
    }
    if (lane < 62) {
#pragma unroll
      for (int i = 0; i < 8; ++i) *(LAS v4u*)(L + L_S + (8 * wv + i) * QPITCH + ((lane ^ (wv >> 1)) * 16)) = qf[i];
    }
  }
  sig(prog, wv, 4u, lane);
  STAMP(5);
  wait4(prog, 0, 4u);
  const v4u q31 = __builtin_amdgcn_raw_buffer_load_b128(rs, WS_QA + ab * 32768 + (lane & 31) * 1024 + 992 + (lane >> 5) * 16, 0, AUX_SC1);
  const int r31 = lane & 31, hh = lane >> 5;
  f32x16 sacc;
#pragma unroll
  for (int i = 0; i < 16; ++i) sacc[i] = 0.f;
  {
    const int row = 32 * wv + r31;
    const int sw = ((row & 3) << 2) | ((row >> 2) & 3);
    LAS unsigned char* arow = L + L_C + row * 1024;
    LAS unsigned char* brow = L + L_S + r31 * QPITCH + ((hh ^ (r31 >> 4)) * 16);
    constexpr int W = 6;
    v4u af[32], bf[32];
#pragma unroll
    for (int s = 0; s < W; ++s) {
      const int c64 = 2 * s + hh;
      af[s] = *(LAS v4u*)(arow + (c64 >> 4) * 256 + (((c64 & 15) ^ sw) * 16));
      bf[s] = *(LAS v4u*)(brow + s * 32);
    }
    __builtin_amdgcn_sched_barrier(0);
#pragma unroll
    for (int s = 0; s < 32; ++s) {
      if (s + W < 32) {
        const int c64 = 2 * (s + W) + hh;
        af[s + W] = *(LAS v4u*)(arow + (c64 >> 4) * 256 + (((c64 & 15) ^ sw) * 16));
        bf[s + W] = s + W < 31 ? *(LAS v4u*)(brow + (s + W) * 32) : q31;
      }
      sacc = __builtin_amdgcn_mfma_f32_32x32x16_bf16(__builtin_bit_cast(bf16x8, af[s]), __builtin_bit_cast(bf16x8, bf[s]), sacc, 0, 0, 0);
      __builtin_amdgcn_sched_barrier(0);
    }
  }
  float mx = sacc[0];
#pragma unroll
  for (int i = 1; i < 16; ++i) mx = fmaxf(mx, sacc[i]);
  mx = xmax32(mx);
  if (lane < 32) *(LAS float*)(L + L_MX + (wv * 32 + lane) * 4) = mx;
  sig(prog, wv, 5u, lane);
  STAMP(17);
  wait4(prog, 0, 5u);
#pragma unroll
  for (int w = 0; w < 4; ++w) mx = fmaxf(mx, *(LAS float*)(L + L_MX + (w * 32 + r31) * 4));
  {
    float ls = 0.f;
    v4u pf[2];
#pragma unroll
    for (int i = 0; i < 8; ++i) {
      const float p0 = __builtin_amdgcn_exp2f(sacc[2 * i] - mx), p1 = __builtin_amdgcn_exp2f(sacc[2 * i + 1] - mx);
      const unsigned pk = cvtpk(p0, p1);
      ls += blo(pk) + bhi(pk);
      pf[i >> 2][i & 3] = pk;
    }
    ls = xsum32(ls);
    *(LAS v4u*)(L + L_P + (wv * 2 + 0) * 1024 + lane * 16) = pf[0];
    *(LAS v4u*)(L + L_P + (wv * 2 + 1) * 1024 + lane * 16) = pf[1];
    if (lane < 32) *(LAS float*)(L + L_LS + (wv * 32 + lane) * 4) = ls;
  }
  sig(prog, wv, 6u, lane);
  wait4(prog, 0, 6u);
  STAMP(7);
  f32x16 oacc[4];
#pragma unroll
  for (int cb = 0; cb < 4; ++cb)
#pragma unroll
    for (int i = 0; i < 16; ++i) oacc[cb][i] = 0.f;
  float lt = 0.f;
  {
    const int blk = (lane >> 4) & 1, q4 = (lane & 15) >> 2, p4 = lane & 3;
    LAS unsigned char* tb = L + L_C + (4 * hh + q4) * 1024 + wv * 256 + 8 * (p4 & 1);
    v4u pb[8];
#pragma unroll
    for (int ks = 0; ks < 8; ++ks) pb[ks] = *(LAS v4u*)(L + L_P + ks * 1024 + lane * 16);
#pragma unroll
    for (int w = 0; w < 4; ++w) lt += *(LAS float*)(L + L_LS + (w * 32 + r31) * 4);
    constexpr int W = 5;
    s16x4 a[32][2];
#define TRLD(n) { _Pragma("unroll") for (int t = 0; t < 2; ++t) { \
      const int chunk = ((((n) & 3) ^ q4) << 2) | ((2 * blk + (p4 >> 1)) ^ (2 * t + hh)); \
      a[n][t] = __builtin_amdgcn_ds_read_tr16_b64_v4i16((LAS s16x4*)(tb + (16 * ((n) >> 2) + 8 * t) * 1024 + chunk * 16)); } }
#pragma unroll
    for (int n = 0; n < W; ++n) TRLD(n)
    __builtin_amdgcn_sched_barrier(0);
#pragma unroll
    for (int n = 0; n < 32; ++n) {
      if (n + W < 32) TRLD(n + W)
      const bf16x8 A = __builtin_shufflevector(a[n][0], a[n][1], 0, 1, 2, 3, 4, 5, 6, 7);
      oacc[n & 3] = __builtin_amdgcn_mfma_f32_32x32x16_bf16(A, __builtin_bit_cast(bf16x8, pb[n >> 2]), oacc[n & 3], 0, 0, 0);
      if (n == 27) sig(prog, wv, 7u, lane);
      __builtin_amdgcn_sched_barrier(0);
    }
#undef TRLD
  }
  STAMP(18);
  {
    const float inv = __builtin_amdgcn_rcpf(lt);
    LAS unsigned char* st = L + L_ST + wv * 4096;
#pragma unroll
    for (int half = 0; half < 2; ++half) {
#pragma unroll
      for (int c2 = 0; c2 < 2; ++c2)
#pragma unroll
        for (int g = 0; g < 4; ++g) {
          const f32x16& o = oacc[2 * half + c2];
          v2u pk;
          pk.x = cvtpk(o[4 * g] * inv, o[4 * g + 1] * inv); pk.y = cvtpk(o[4 * g + 2] * inv, o[4 * g + 3] * inv);
          *(LAS v2u*)(st + r31 * 128 + (((4 * c2 + g) ^ (r31 & 7)) * 16) + hh * 8) = pk;
        }
      waitl();
      v4u ov[4];
#pragma unroll
      for (int i = 0; i < 4; ++i) { const int hd = 8 * i + (lane >> 3); ov[i] = *(LAS v4u*)(st + hd * 128 + (((lane & 7) ^ (hd & 7)) * 16)); }
#pragma unroll
      for (int i = 0; i < 4; ++i) {
        const int hd = 8 * i + (lane >> 3);
        __builtin_amdgcn_raw_buffer_store_b128(ov[i], rs, WS_PART + (((hd * 8 + 2 * wv + half) * 2 + ab) * 128 + asplit) * 128 + (lane & 7) * 16, 0, AUX_SC1);
      }
      cfence();
    }
    sig(prog, wv, 8u, lane);
    STAMP(8);
    const unsigned lu = sanef(mx + __builtin_amdgcn_logf(lt));
    wait1(prog, 12, 1u);
    waitv<0>();
    if (lane < 32) __builtin_amdgcn_raw_buffer_store_b128(v4u{lu, lu, lu, lu}, rs, WS_LSE + (((lane * 4 + wv) * 2 + ab) * 128 + asplit) * 16, 0, AUX_SC1);
  }
  STAMP(9);
  const int tq = ch >> 2, tp = lane & 3;
  const int trow = 8 * rg + tq, tcol = (tp >> 1) * 16 + (tp & 1) * 8;
  const f32x4 z4 = {0.f, 0.f, 0.f, 0.f};
  wait4(prog, 4, 3u); wait4(prog, 8, 3u);
  bf16x8 auv[2][2], awo[4], awo4[4];
#pragma unroll
  for (int ks = 0; ks < 2; ++ks)
#pragma unroll
    for (int t = 0; t < 2; ++t) auv[ks][t] = wfrag(L + I_UV + (32 * ks + trow) * 256 + (2 * wv + t) * 32 + tcol, 256);
#pragma unroll
  for (int ks = 0; ks < 4; ++ks) {
    awo[ks] = wfrag(L + I_WO + (32 * ks + trow) * 144 + wv * 32 + tcol, 144);
    awo4[ks] = wfrag(L + I_WO + (32 * ks + trow) * 144 + 128 + tcol, 144);
  }
  {
    const int rbase = WS_PART + ((h * 8 + cs) * 2 + (wv >> 1)) * 128 * 128;
    const int lbase = WS_LSE + ((h * 4 + (cs >> 1)) * 2 + (wv >> 1)) * 2048 + lane * 16;
    unsigned l0, l1;
    for (unsigned spin = 0; spin < SPIN_CAP; ++spin) {
      l0 = __builtin_amdgcn_raw_buffer_load_b32(rs, lbase, 0, AUX_SC1);
      l1 = __builtin_amdgcn_raw_buffer_load_b32(rs, lbase + 1024, 0, AUX_SC1);
      cfence();
      if (__all(max(l0, l1) != SENT)) break;
      __builtin_amdgcn_s_sleep(1);
    }
    cfence();
    STAMP(10);
    v4u pv[8];
    const int pbase = rbase + 64 * (wv & 1) * 128 + lane * 16;
#pragma unroll
    for (int i = 0; i < 8; ++i) pv[i] = __builtin_amdgcn_raw_buffer_load_b128(rs, pbase + i * 1024, 0, AUX_SC1);
    cfence();
    if (wv == 3) sig(prog, 13, 1u, lane);
    const float m = xmax32(xmax16(max_row16(fmaxf(asf(l0), asf(l1)))));
    const float e0 = __builtin_amdgcn_exp2f(asf(l0) - m), e1 = __builtin_amdgcn_exp2f(asf(l1) - m);
    const float it = __builtin_amdgcn_rcpf(wave_sum(e0 + e1));
    wait4(prog, 0, 8u);
    *(LAS float*)(L + T_WG + wv * 256 + lane * 4) = ((wv & 1) ? e1 : e0) * it;
    waitl();
    float acc[8];
#pragma unroll
    for (int k = 0; k < 8; ++k) acc[k] = 0.f;
#pragma unroll
    for (int i = 0; i < 8; ++i) {
      const float w = *(LAS float*)(L + T_WG + wv * 256 + (8 * i + (lane >> 3)) * 4);
#pragma unroll
      for (int k = 0; k < 4; ++k) { acc[2 * k] = fmaf(w, blo(pv[i][k]), acc[2 * k]); acc[2 * k + 1] = fmaf(w, bhi(pv[i][k]), acc[2 * k + 1]); }
    }
    float t[4], u2[2];
#pragma unroll
    for (int k = 0; k < 4; ++k) t[k] = tr32(acc[k], acc[k + 4]);
#pragma unroll
    for (int k = 0; k < 2; ++k) u2[k] = tr16(t[k], t[k + 2]);
    u2[0] += dpp<ROR8>(u2[0]); u2[1] += dpp<ROR8>(u2[1]);
    if ((lane & 8) == 0) *(LAS f32x2*)(L + T_CC + (wv * 64 + (lane & 7) * 8 + (lane >> 5) * 4 + ((lane >> 4) & 1) * 2) * 4) = f32x2{u2[0], u2[1]};
  }
  sig(prog, wv, 9u, lane);
  STAMP(11);
  wait4(prog, 0, 9u);
  {
    f32x4 c[2][4];
#pragma unroll
    for (int ks = 0; ks < 2; ++ks) {
      LAS unsigned char* cp = L + T_CC + ((ch & 1) * 128 + 32 * ks + 8 * rg) * 4;
      c[ks][0] = *(LAS f32x4*)(cp); c[ks][1] = *(LAS f32x4*)(cp + 256); c[ks][2] = *(LAS f32x4*)(cp + 16); c[ks][3] = *(LAS f32x4*)(cp + 272);
    }
    f32x4 d[2] = {z4, z4};
#pragma unroll
    for (int ks = 0; ks < 2; ++ks) {
      v2u h0, l0, h1, l1;
      split4(c[ks][0] + c[ks][1], h0, l0); split4(c[ks][2] + c[ks][3], h1, l1);
      const v4u b = (ch & 2) ? v4u{l0.x, l0.y, l1.x, l1.y} : v4u{h0.x, h0.y, h1.x, h1.y};
#pragma unroll
      for (int t = 0; t < 2; ++t) d[t] = mfma16(auv[ks][t], b, d[t]);
    }
#pragma unroll
    for (int t = 0; t < 2; ++t) publish4(rs, ch < 2 ? WS_CX + (h * 8 + cs) * 1024 + (ch * 128 + 32 * wv + 16 * t + 4 * rg) * 4 : OOB, hilo(d[t]));
  }
  STAMP(19);
  if (wv == 0) {
    const f32x4 cx = gather8(rs, WS_CX + h * 8192, lane);
    v2u hi, lo;
    split4(cx, hi, lo);
    *(LAS v2u*)(L + T_CX + hh * 272 + r31 * 8) = hi;
    *(LAS v2u*)(L + T_CX + (2 + hh) * 272 + r31 * 8) = lo;
    sig(prog, 14, 3u, lane);
  } else {
    wait1(prog, 14, 3u);
  }
  STAMP(20);
  {
    v4u b[4];
#pragma unroll
    for (int ks = 0; ks < 4; ++ks) b[ks] = *(LAS v4u*)(L + T_CX + (ch & 3) * 272 + (32 * ks + 8 * rg) * 2);
    f32x4 d = z4, d4 = z4;
#pragma unroll
    for (int ks = 0; ks < 4; ++ks) { d = mfma16(awo[ks], b[ks], d); d4 = mfma16(awo4[ks], b[ks], d4); }
    const int o = ch * 576 + 72 * cs + 16 * wv + 4 * rg, o4 = ch * 576 + 72 * cs + 64 + 4 * rg;
    publish4(rs, ch < 2 ? WS_OP + ((o >> 4) * 32 + h) * 64 + (o & 15) * 4 : OOB, hilo(d));
    publish4(rs, (ch < 2 && rg < 2 && wv == 2) ? WS_OP + ((o4 >> 4) * 32 + h) * 64 + (o4 & 15) * 4 : OOB, hilo(d4));
  }
  STAMP(12);
  if (owner) {
    if (wv < 2) {
      const v2u xv = *(const v2u*)(x + own * 16 + (lane & 3) * 4);
      const f32x4 s = own_reduce<2>(rs, WS_OP + own * 2048, lane);
      const f32x4 hown = f32x4{blo(xv.x) + s[0], bhi(xv.x) + s[1], blo(xv.y) + s[2], bhi(xv.y) + s[3]};
      if (lane < 4) publish4(rs, WS_HB + own * 64 + lane * 16, hown);
      STAMP(21);
      const f32x4 s2 = own_reduce<14>(rs, WS_DP + own * (NFF * 64), lane);
      v2u o;
      o.x = cvtpk(hown[0] + s2[0], hown[1] + s2[1]);
      o.y = cvtpk(hown[2] + s2[2], hown[3] + s2[3]);
      if (lane < 4) *(v2u*)(out + own * 16 + lane * 4) = o;
      if (own == 0 && lane == 0)
        __hip_atomic_store((u64*)(ws + WS_EPOCH), ep + 1ull, __ATOMIC_RELAXED, __HIP_MEMORY_SCOPE_AGENT);
      STAMP(15);
    }
  } else if (ffn) {
    wait4(prog, 4, 4u); wait4(prog, 8, 4u);
    bf16x8 ag[6], au[6], wa[9];
    {
      LAS unsigned char* ap = L + I_GU + trow * 272 + wv * 32 + tcol;
#pragma unroll
      for (int ks = 0; ks < 6; ++ks) { ag[ks] = wfrag(ap + ks * 32 * 272, 272); au[ks] = wfrag(ap + ks * 32 * 272 + 128, 272); }
#pragma unroll
      for (int t = 0; t < 9; ++t) wa[t] = wfrag(L + I_DN + trow * 1168 + (9 * wv + t) * 32 + tcol, 1168);
    }
    if (wv == 0) {
      v4u hv[5];
      for (unsigned spin = 0; spin < SPIN_CAP; ++spin) {
#pragma unroll
        for (int k = 0; k < 4; ++k) hv[k] = __builtin_amdgcn_raw_buffer_load_b128(rs, WS_HB + k * 1024 + lane * 16, 0, AUX_SC1);
        hv[4] = __builtin_amdgcn_raw_buffer_load_b128(rs, WS_HB + 4096 + (lane & 31) * 16, 0, AUX_SC1);
        cfence();
        unsigned m = max4(hv[0]);
#pragma unroll
        for (int k = 1; k < 5; ++k) m = max(m, max4(hv[k]));
        if (__all(m != SENT)) break;
      }
      STAMP(13);
      f32x4 f[5];
#pragma unroll
      for (int k = 0; k < 5; ++k) f[k] = __builtin_bit_cast(f32x4, hv[k]);
      const bool m2 = lane < 16, m4 = lane < 32;
      float s[5];
#pragma unroll
      for (int k = 0; k < 5; ++k) s[k] = (f[k][0] + f[k][1]) + (f[k][2] + f[k][3]);
      const float m0 = wave_sum(s[0] + s[1] + (m2 ? s[2] : 0.f)) * (1.0f / DM);
      const float m1 = wave_sum((m2 ? 0.f : s[2]) + s[3] + (m4 ? s[4] : 0.f)) * (1.0f / DM);
      const float mm[5] = {m0, m0, m2 ? m0 : m1, m1, m1};
#pragma unroll
      for (int k = 0; k < 5; ++k) { f[k] -= mm[k]; s[k] = (f[k][0] * f[k][0] + f[k][1] * f[k][1]) + (f[k][2] * f[k][2] + f[k][3] * f[k][3]); }
      const float v0 = wave_sum(s[0] + s[1] + (m2 ? s[2] : 0.f)) * (1.0f / DM);
      const float v1 = wave_sum((m2 ? 0.f : s[2]) + s[3] + (m4 ? s[4] : 0.f)) * (1.0f / DM);
      const float r0 = __builtin_amdgcn_rsqf(v0 + 1e-5f), r1 = __builtin_amdgcn_rsqf(v1 + 1e-5f);
      const float rr[5] = {r0, r0, m2 ? r0 : r1, r1, r1};
#pragma unroll
      for (int k = 0; k < 5; ++k) {
        const int i4 = 64 * k + lane;
        if (k < 4 || m4) {
          const int b = i4 >= 144 ? 1 : 0, m = (i4 - 144 * b) * 4;
          v2u hi, lo;
          split4(f[k] * rr[k], hi, lo);
          *(LAS v2u*)(L + T_XN + b * 1168 + m * 2) = hi;
          *(LAS v2u*)(L + T_XN + (2 + b) * 1168 + m * 2) = lo;
        }
      }
      sig(prog, 14, 4u, lane);
      STAMP(22);
    } else {
      wait1(prog, 14, 4u);
    }
    {
      f32x4 dg_ = z4, du_ = z4;
      LAS unsigned char* bp = L + T_XN + (ch & 3) * 1168 + (192 * kt + 8 * rg) * 2;
      v4u b[6];
#pragma unroll
      for (int ks = 0; ks < 6; ++ks) b[ks] = *(LAS v4u*)(bp + ks * 64);
#pragma unroll
      for (int ks = 0; ks < 6; ++ks) { dg_ = mfma16(ag[ks], b[ks], dg_); du_ = mfma16(au[ks], b[ks], du_); }
      const int ob = ch < 2 ? WS_GX + fi * 1024 + (ch * 64 + 16 * wv + 4 * rg) * 4 : OOB;
      publish4(rs, ob, hilo(dg_));
      publish4(rs, ob + 512, hilo(du_));
    }
    STAMP(23);
    if (wv == 0) {
      v4u pv[3];
      poll_recs<3>(pv, rs, WS_GX + cb * 3072 + lane * 16);
      const f32x4 sgu = __builtin_bit_cast(f32x4, pv[0]) + __builtin_bit_cast(f32x4, pv[1]) + __builtin_bit_cast(f32x4, pv[2]);
      f32x4 a;
#pragma unroll
      for (int k = 0; k < 4; ++k) {
        auto t = __builtin_amdgcn_permlane32_swap(asu(sgu[k]), asu(sgu[k]), false, false);
        const float g = asf(t[0]), u = asf(t[1]);
        a[k] = g * __builtin_amdgcn_rcpf(1.0f + __expf(-g)) * u;
      }
      v2u hi, lo;
      split4(a, hi, lo);
      *(LAS v2u*)(L + T_ACT + (2 * hh + ((lane >> 4) & 1)) * 176 + ch * 8) = hh ? lo : hi;
      if (lane < 16) *(LAS v2u*)(L + T_ACT + (lane >> 2) * 176 + 128 + (lane & 3) * 8) = v2u{0u, 0u};
      sig(prog, 14, 5u, lane);
    } else {
      wait1(prog, 14, 5u);
    }
    STAMP(25);
    {
      const v4u b = *(LAS v4u*)(L + T_ACT + (ch & 3) * 176 + (24 * kt + 8 * rg) * 2);
      const int ob = ch < 2 ? WS_DP + ((ch * 36 + 9 * wv) * NFF + fi) * 64 + rg * 16 : OOB;
      f32x4 d[9];
#pragma unroll
      for (int t = 0; t < 9; ++t) d[t] = mfma16(wa[t], b, z4);
#pragma unroll
      for (int t = 0; t < 9; ++t) publish4(rs, ob + t * (NFF * 64), hilo(d[t]));
    }
    STAMP(14);
  }
  STAMP_FLUSH();
}
}

extern "C" void kernel_launch(void* const* d_in, const int* in_sizes, int n_in,
                              void* d_out, int out_size, void* d_ws, size_t ws_size,
                              hipStream_t stream) {
  using mk::u16;
  (void)hipFuncSetAttribute((const void*)mk::fused, hipFuncAttributeMaxDynamicSharedMemorySize, mk::L_BYTES);
  mk::fused<<<mk::NWG, mk::NT, mk::L_BYTES, stream>>>(
      (const u16*)d_in[0], (const u16*)d_in[1], (const u16*)d_in[2], (const u16*)d_in[3], (const u16*)d_in[4], (const u16*)d_in[5],
      (const u16*)d_in[7], (const u16*)d_in[8], (const u16*)d_in[9], (u16*)d_out, (unsigned char*)d_ws);
}
```

```cpp
#include <hip/hip_runtime.h>
#include <hip/hip_bf16.h>
#include <stdint.h>

#define MK_STAMPS 0
#define DMA_DEPTH 16

#define GLOAD_WQ gload4nt
#define SL0 4
#define SL1 4
#define TAIL_AT 0
#define TAIL_PACE 63
#define PACE() waitv<TAIL_PACE - 1>()
#define NB1 4
#define DMA_D0 2
#define DMA_SW 1u
#define B2_AT 2
namespace mk {
constexpr int SK = 16384, DM = 576;
constexpr int NWG = 256, NT = 768;
constexpr int NOWN = 72;
constexpr int NFF = 216;

constexpr int WS_EPOCH = 0;
constexpr int WS_FLAG = 4096, NREP = 8;
constexpr int WS_QA = 65536;
constexpr int WS_Q0 = WS_QA + 65536;
constexpr int WS_T0 = WS_Q0 + 262144;
constexpr int WS_T1 = WS_T0 + 32768;
constexpr int WS_CLR = WS_T1 + NREP * 8192;
constexpr int WS_HB = WS_CLR;
constexpr int WS_CX = WS_HB + 8192;
constexpr int WS_OP = WS_CX + 262144;
constexpr int WS_DP = WS_OP + NOWN * 32 * 64;
constexpr int WS_GX = WS_DP + NOWN * NFF * 64;
constexpr int WS_LSE = WS_GX + NFF * 1024;
constexpr int WS_CLR_END = WS_LSE + 32 * 4 * 256 * 16;
constexpr int CLR_PER_WG = (WS_CLR_END - WS_CLR) / NWG;
static_assert(CLR_PER_WG * NWG == WS_CLR_END - WS_CLR && CLR_PER_WG % 16 == 0 && CLR_PER_WG <= 9 * 1024, "clear split");
constexpr int WS_PART = 4 << 20;
constexpr int WS_STAMP = 24 << 20;
constexpr int WS_BYTES = 32 << 20;

constexpr int L_C = 0;
constexpr int L_S = 131072;
constexpr int L_CTRL = 162816;
constexpr int L_BYTES = 163840;
constexpr int L_XL = L_S;
constexpr int L_QP = L_S + 5120;
constexpr int L_QV = L_S + 13312;
constexpr int L_QO = L_S + 14336;
constexpr int QPITCH = 992;
constexpr int L_P = L_S;
constexpr int L_LS = L_S + 8192;
constexpr int L_MX = L_CTRL + 128;
constexpr int I_UV = 0;
constexpr int I_WO = 16384;
constexpr int I_GU = 34944;
constexpr int I_DN = 88064;
constexpr int L_ST = 145536;
constexpr int T_WG = L_ST;
constexpr int T_CC = L_ST + 1024;
constexpr int T_CX = L_ST + 2048;
constexpr int T_XN = L_ST + 4096;
constexpr int T_ACT = L_ST + 9216;
static_assert(I_GU + 192 * 272 <= I_DN && I_DN + 32 * 1168 <= L_S && L_ST + 16384 <= L_CTRL, "LDS map");

#define LAS __attribute__((address_space(3)))
typedef unsigned v4u __attribute__((ext_vector_type(4)));
typedef unsigned v2u __attribute__((ext_vector_type(2)));
typedef float f32x4 __attribute__((ext_vector_type(4)));
typedef float f32x2 __attribute__((ext_vector_type(2)));
typedef float f32x16 __attribute__((ext_vector_type(16)));
typedef short bf16x8 __attribute__((ext_vector_type(8)));
typedef short s16x4 __attribute__((ext_vector_type(4)));
typedef unsigned short u16;
typedef unsigned long long u64;
#define DI __device__ __forceinline__
constexpr int AUX_SC1 = 16;
constexpr unsigned SENT = 0xFFFFFFFFu;
constexpr unsigned SPIN_CAP = 1u << 16;

DI float asf(unsigned u) { return __builtin_bit_cast(float, u); }
DI unsigned asu(float f) { return __builtin_bit_cast(unsigned, f); }
DI float blo(unsigned u) { return asf(u << 16); }
DI float bhi(unsigned u) { return asf(u & 0xffff0000u); }
DI unsigned cvtpk(float lo, float hi) { unsigned r; asm("v_cvt_pk_bf16_f32 %0, %1, %2" : "=v"(r) : "v"(lo), "v"(hi)); return r; }
DI unsigned sane(unsigned u) { return u == SENT ? 0x7FC07FC0u : u; }
DI unsigned sanef(float f) { unsigned u = asu(f); return u == SENT ? 0x7FC00000u : u; }
template <int CTRL> DI float dpp(float x) { return asf((unsigned)__builtin_amdgcn_mov_dpp((int)asu(x), CTRL, 0xf, 0xf, true)); }
constexpr int XOR1 = 0xB1, XOR2 = 0x4E, HMIR = 0x141, MIR = 0x140, ROR4 = 0x124, ROR8 = 0x128;
DI float sum_row16(float x) { x += dpp<XOR1>(x); x += dpp<XOR2>(x); x += dpp<HMIR>(x); x += dpp<MIR>(x); return x; }
DI float max_row16(float x) { x = fmaxf(x, dpp<XOR1>(x)); x = fmaxf(x, dpp<XOR2>(x)); x = fmaxf(x, dpp<HMIR>(x)); x = fmaxf(x, dpp<MIR>(x)); return x; }
DI float xsum16(float x) { auto s = __builtin_amdgcn_permlane16_swap(asu(x), asu(x), false, false); return asf(s[0]) + asf(s[1]); }
DI float xsum32(float x) { auto s = __builtin_amdgcn_permlane32_swap(asu(x), asu(x), false, false); return asf(s[0]) + asf(s[1]); }
DI float xmax16(float x) { auto s = __builtin_amdgcn_permlane16_swap(asu(x), asu(x), false, false); return fmaxf(asf(s[0]), asf(s[1])); }
DI float xmax32(float x) { auto s = __builtin_amdgcn_permlane32_swap(asu(x), asu(x), false, false); return fmaxf(asf(s[0]), asf(s[1])); }
DI float tr32(float a, float b) { auto s = __builtin_amdgcn_permlane32_swap(asu(a), asu(b), false, false); return asf(s[0]) + asf(s[1]); }
DI float tr16(float a, float b) { auto s = __builtin_amdgcn_permlane16_swap(asu(a), asu(b), false, false); return asf(s[0]) + asf(s[1]); }
template <int CTRL> DI unsigned dppu(unsigned x) { return (unsigned)__builtin_amdgcn_mov_dpp((int)x, CTRL, 0xf, 0xf, true); }
DI unsigned xor4(const v4u& a) { return (a.x ^ a.y) ^ (a.z ^ a.w); }
DI unsigned add4(const v4u& a) { return (a.x + a.y) + (a.z + a.w); }
DI unsigned wave_xor(unsigned x) {
  x ^= dppu<XOR1>(x); x ^= dppu<XOR2>(x); x ^= dppu<HMIR>(x); x ^= dppu<MIR>(x);
  auto s = __builtin_amdgcn_permlane16_swap(x, x, false, false); x = s[0] ^ s[1];
  auto t = __builtin_amdgcn_permlane32_swap(x, x, false, false); return t[0] ^ t[1];
}
DI unsigned wave_add(unsigned x) {
  x += dppu<XOR1>(x); x += dppu<XOR2>(x); x += dppu<HMIR>(x); x += dppu<MIR>(x);
  auto s = __builtin_amdgcn_permlane16_swap(x, x, false, false); x = s[0] + s[1];
  auto t = __builtin_amdgcn_permlane32_swap(x, x, false, false); return t[0] + t[1];
}
template <bool ADD> DI unsigned tred8(const unsigned (&v)[8], int lane) {
  const bool b2 = lane & 4, b1 = lane & 2, b0 = lane & 1;
  unsigned a[4], b[2];
#pragma unroll
  for (int k = 0; k < 4; ++k) { const unsigned keep = b2 ? v[k + 4] : v[k], send = b2 ? v[k] : v[k + 4], r = dppu<HMIR>(send); a[k] = ADD ? keep + r : keep ^ r; }
#pragma unroll
  for (int k = 0; k < 2; ++k) { const unsigned keep = b1 ? a[k + 2] : a[k], send = b1 ? a[k] : a[k + 2], r = dppu<XOR2>(send); b[k] = ADD ? keep + r : keep ^ r; }
  const unsigned keep = b0 ? b[1] : b[0], send = b0 ? b[0] : b[1], r = dppu<XOR1>(send);
  return ADD ? keep + r : keep ^ r;
}
DI float wave_sum(float x) { return xsum32(xsum16(sum_row16(x))); }

template <int N> DI void waitv() { asm volatile("s_waitcnt vmcnt(%0)" ::"i"(N) : "memory"); }
DI void waitl() { asm volatile("s_waitcnt lgkmcnt(0)" ::: "memory"); }
DI void cfence() { asm volatile("" ::: "memory"); }
DI void bar() { asm volatile("s_waitcnt lgkmcnt(0)\n\ts_barrier" ::: "memory"); }
#define TIE(x) asm volatile("" : "+v"(x))
#define TIE8(a) asm volatile("" : "+v"(a[0]), "+v"(a[1]), "+v"(a[2]), "+v"(a[3]), "+v"(a[4]), "+v"(a[5]), "+v"(a[6]), "+v"(a[7]))

DI void gload4(v4u& d, const void* sb, unsigned vo) { asm volatile("global_load_dwordx4 %0, %1, %2" : "=v"(d) : "v"(vo), "s"(sb) : "memory"); }
DI void gload4nt(v4u& d, const void* sb, unsigned vo) { asm volatile("global_load_dwordx4 %0, %1, %2 nt" : "=v"(d) : "v"(vo), "s"(sb) : "memory"); }
DI void gload1p(unsigned& d, const void* p) { asm volatile("global_load_dword %0, %1, off nt" : "=v"(d) : "v"(p) : "memory"); }
DI void gloadh(unsigned& d, const void* sb, unsigned vo) { asm volatile("global_load_ushort %0, %1, %2" : "=v"(d) : "v"(vo), "s"(sb) : "memory"); }
DI void gload2sc(u64& d, const void* sb, unsigned vo) { asm volatile("global_load_dwordx2 %0, %1, %2 sc1" : "=v"(d) : "v"(vo), "s"(sb) : "memory"); }
DI void gstore4sc(const v4u& d, void* sb, unsigned vo) { asm volatile("global_store_dwordx4 %0, %1, %2 sc1\n\ts_nop 1" ::"v"(vo), "v"(d), "s"(sb) : "memory"); }
DI void gstore2sc(const u64& d, void* sb, unsigned vo) { asm volatile("global_store_dwordx2 %0, %1, %2 sc1\n\ts_nop 1" ::"v"(vo), "v"(d), "s"(sb) : "memory"); }
DI void dma16(const void* sb, unsigned vo, unsigned ldsaddr) {
  unsigned keep;
  asm volatile("s_mov_b32 %0, m0\n\ts_mov_b32 m0, %3\n\ts_nop 0\n\tglobal_load_lds_dwordx4 %1, %2 nt\n\ts_mov_b32 m0, %0"
               : "=&s"(keep) : "v"(vo), "s"(sb), "s"(ldsaddr) : "memory");
}

#define STAMP(k) do { } while (0)
#define STAMP_FLUSH() do { } while (0)
#define STAMP_DECL

DI void sig(LAS unsigned* prog, int idx, unsigned v, int lane) {
  waitl();
  if (lane == 0) *(volatile LAS unsigned*)(prog + idx) = v;
  cfence();
}
DI void wait4(LAS unsigned* prog, int first, unsigned v) {
  for (;;) {
    v4u a = *(volatile LAS v4u*)(prog + first);
    unsigned mn = min(min(a.x, a.y), min(a.z, a.w));
    if (__builtin_amdgcn_readfirstlane(mn) >= v) break;
  }
  cfence();
}
DI void wait1(LAS unsigned* prog, int idx, unsigned v) {
  for (;;) {
    unsigned a = *(volatile LAS unsigned*)(prog + idx);
    if (__builtin_amdgcn_readfirstlane(a) >= v) break;
  }
  cfence();
}
DI unsigned max4(const v4u& a) { return max(max(a.x, a.y), max(a.z, a.w)); }

DI void fma8(float (&a0)[8], float (&a1)[8], const v4u& w, float x0, float x1) {
#pragma unroll
  for (int k = 0; k < 4; ++k) {
    const float lo = blo(w[k]), hi = bhi(w[k]);
    a0[2 * k] = fmaf(lo, x0, a0[2 * k]); a1[2 * k] = fmaf(lo, x1, a1[2 * k]);
    a0[2 * k + 1] = fmaf(hi, x0, a0[2 * k + 1]); a1[2 * k + 1] = fmaf(hi, x1, a1[2 * k + 1]);
  }
}

constexpr int OOB = 0x70000000;
template <int N> DI void poll_recs(v4u (&pv)[N], __amdgpu_buffer_rsrc_t rs, int base, bool hiHalf = false) {
  for (unsigned spin = 0; spin < SPIN_CAP; ++spin) {
#pragma unroll
    for (int i = 0; i < N; ++i) pv[i] = __builtin_amdgcn_raw_buffer_load_b128(rs, (N == 14 && i == 13 && hiHalf) ? OOB : base + i * 1024, 0, AUX_SC1);
    cfence();
    unsigned m = max4(pv[0]);
#pragma unroll
    for (int i = 1; i < N; ++i) m = max(m, max4(pv[i]));
    if (__all(m != SENT)) break;
  }
}
template <int N> DI f32x4 own_reduce(__amdgpu_buffer_rsrc_t rs, int base, int lane) {
  v4u pv[N];
  poll_recs<N>(pv, rs, base + lane * 16, lane >= 32);
  f32x4 s = __builtin_bit_cast(f32x4, pv[0]);
#pragma unroll
  for (int i = 1; i < N; ++i) s += __builtin_bit_cast(f32x4, pv[i]);
#pragma unroll
  for (int k = 0; k < 4; ++k) { float t = s[k]; t += dpp<ROR8>(t); t += dpp<ROR4>(t); s[k] = xsum32(xsum16(t)); }
  return s;
}
DI f32x4 gather8(__amdgpu_buffer_rsrc_t rs, int base, int lane) {
  v4u pv[8];
  poll_recs<8>(pv, rs, base + lane * 16);
  f32x4 s = __builtin_bit_cast(f32x4, pv[0]);
#pragma unroll
  for (int i = 1; i < 8; ++i) s += __builtin_bit_cast(f32x4, pv[i]);
  return s;
}
DI void publish4(__amdgpu_buffer_rsrc_t rs, int off, const f32x4& s) {
  const v4u o = {sanef(s[0]), sanef(s[1]), sanef(s[2]), sanef(s[3])};
  __builtin_amdgcn_raw_buffer_store_b128(o, rs, off, 0, AUX_SC1);
}
DI void split4(const f32x4& v, v2u& hi, v2u& lo) {
  hi.x = cvtpk(v[0], v[1]); hi.y = cvtpk(v[2], v[3]);
  lo.x = cvtpk(v[0] - blo(hi.x), v[1] - bhi(hi.x)); lo.y = cvtpk(v[2] - blo(hi.y), v[3] - bhi(hi.y));
}
DI bf16x8 wfrag(LAS unsigned char* p, int pitch) {
  const s16x4 a = __builtin_amdgcn_ds_read_tr16_b64_v4i16((LAS s16x4*)p);
  const s16x4 b = __builtin_amdgcn_ds_read_tr16_b64_v4i16((LAS s16x4*)(p + 4 * pitch));
  return __builtin_shufflevector(a, b, 0, 1, 2, 3, 4, 5, 6, 7);
}
DI f32x4 mfma16(const bf16x8& a, const v4u& b, const f32x4& c) {
  return __builtin_amdgcn_mfma_f32_16x16x32_bf16(a, __builtin_bit_cast(bf16x8, b), c, 0, 0, 0);
}
DI f32x4 hilo(const f32x4& d) { f32x4 r; _Pragma("unroll") for (int k = 0; k < 4; ++k) r[k] = d[k] + dpp<XOR2>(d[k]); return r; }

__global__ void __launch_bounds__(NT, 3)
fused(const u16* __restrict__ x, const u16* __restrict__ ckv, const u16* __restrict__ wq, const u16* __restrict__ wuk,
      const u16* __restrict__ wuv, const u16* __restrict__ wo, const u16* __restrict__ wgt, const u16* __restrict__ wup,
      const u16* __restrict__ wdn, u16* __restrict__ out, unsigned char* __restrict__ ws) {
  extern __shared__ __attribute__((aligned(16))) unsigned char smem[];
  LAS unsigned char* L = (LAS unsigned char*)smem;
  LAS unsigned* prog = (LAS unsigned*)(L + L_CTRL);
  const int tid = threadIdx.x, lane = tid & 63, wv = __builtin_amdgcn_readfirstlane(tid >> 6);
  const int wg = blockIdx.x, xcd = wg & 7, jj = wg >> 3;
  const int h = xcd * 4 + (jj >> 3), cs = jj & 7;
  const int ab = wg >> 7, asplit = wg & 127;
  const bool ffn = jj < 27;
  const int cb = xcd * 9 + min(jj / 3, 8), kt = jj % 3, fi = cb * 3 + kt;
  const int nd = kt < 2 ? 24 : 16;
  const bool owner = jj >= 27 && (jj - 27) * 8 + xcd < 36;
  const int own = ((jj - 27) * 8 + xcd) * 2 + (wv & 1);
  const int rg = lane >> 4, ch = lane & 15;
  const __amdgpu_buffer_rsrc_t rs = __builtin_amdgcn_make_buffer_rsrc((void*)ws, 0, WS_BYTES, 0x00020000);
  const bool isC = wv < 4, isAdm = wv == 4, isDma = wv >= 8;
  STAMP_DECL;
  STAMP(0);
  if (tid < 32) prog[tid] = 0u;
  bar();

  u64 ep = 0;
  v4u xa, xb, xc, wqv[5], wukv[4]; unsigned xo0, xo1;
  if (isAdm) {
    gload2sc(ep, ws, WS_EPOCH);
  } else if (isC) {
    const int m0 = 72 * cs + 18 * wv;
    gload2sc(ep, ws, WS_EPOCH);
    gload4(xa, x, lane * 16);
    gload4(xb, x, 1024 + lane * 16);
    gload4(xc, x, 2048 + (lane & 15) * 16);
    gloadh(xo0, x, (m0 + min(lane, 17)) * 2);
    gloadh(xo1, x, 1152 + (m0 + min(lane, 17)) * 2);
    const unsigned char* b = (const unsigned char*)wq + (size_t)m0 * 8192 + h * 256;
#pragma unroll
    for (int i = 0; i < 4; ++i) gload4nt(wqv[i], b + i * 4 * 8192, rg * 8192 + ch * 16);
    gload4nt(wqv[4], b, (16 + (rg & 1)) * 8192 + ch * 16);
    const unsigned char* b2 = (const unsigned char*)wuk + (size_t)(64 * cs + 16 * wv) * 8192 + h * 256;
#pragma unroll
    for (int i = 0; i < 4; ++i) gload4nt(wukv[i], b2 + i * 8192, (4 * rg) * 8192 + ch * 16);
  }
  if (isC) {
    waitv<9>();
    TIE(xa); TIE(xb); TIE(xc); TIE(xo0); TIE(xo1);
    STAMP(1);
    float sa = 0.f, sb = 0.f, sc = 0.f;
#pragma unroll
    for (int k = 0; k < 4; ++k) { sa += blo(xa[k]) + bhi(xa[k]); sb += blo(xb[k]) + bhi(xb[k]); sc += blo(xc[k]) + bhi(xc[k]); }
    const bool b0 = lane < 8, c1 = lane < 16;
    const float m0 = wave_sum(sa + (b0 ? sb : 0.f)) * (1.0f / DM);
    const float m1 = wave_sum((b0 ? 0.f : sb) + (c1 ? sc : 0.f)) * (1.0f / DM);
    const float mb = b0 ? m0 : m1;
    float va = 0.f, vb = 0.f, vc = 0.f;
#pragma unroll
    for (int k = 0; k < 4; ++k) {
      float d;
      d = blo(xa[k]) - m0; va += d * d; d = bhi(xa[k]) - m0; va += d * d;
      d = blo(xb[k]) - mb; vb += d * d; d = bhi(xb[k]) - mb; vb += d * d;
      d = blo(xc[k]) - m1; vc += d * d; d = bhi(xc[k]) - m1; vc += d * d;
    }
    const float v0 = wave_sum(va + (b0 ? vb : 0.f)) * (1.0f / DM);
    const float v1 = wave_sum((b0 ? 0.f : vb) + (c1 ? vc : 0.f)) * (1.0f / DM);
    const float r0 = __builtin_amdgcn_rsqf(v0 + 1e-5f), r1 = __builtin_amdgcn_rsqf(v1 + 1e-5f);
    if (lane < 20) *(LAS f32x2*)(L + L_XL + (wv * 20 + lane) * 8) = lane < 18 ? f32x2{(blo(xo0) - m0) * r0, (blo(xo1) - m1) * r1} : f32x2{0.f, 0.f};
    waitl();
    f32x2 xr[5];
#pragma unroll
    for (int i = 0; i < 5; ++i) xr[i] = *(LAS f32x2*)(L + L_XL + (wv * 20 + 4 * i + rg) * 8);
    float a0[8], a1[8];
#pragma unroll
    for (int k = 0; k < 8; ++k) { a0[k] = 0.f; a1[k] = 0.f; }
    waitv<4>();
#pragma unroll
    for (int i = 0; i < 5; ++i) { TIE(wqv[i]); fma8(a0, a1, wqv[i], xr[i][0], xr[i][1]); }
    float t[8];
#pragma unroll
    for (int k = 0; k < 8; ++k) t[k] = tr32(a0[k], a1[k]);
    f32x4 o;
#pragma unroll
    for (int k = 0; k < 4; ++k) o[k] = tr16(t[k], t[k + 4]);
    *(LAS f32x4*)(L + L_QP + (wv * 256 + (lane >> 5) * 128 + ch * 8 + (rg & 1) * 4) * 4) = o;
    sig(prog, wv, 1u, lane);
    STAMP(2);
  }

  if (!isC) {
    if (isAdm) {
      const v4u sv = {SENT, SENT, SENT, SENT};
#pragma unroll
      for (int i = 0; i < 9; ++i) gstore4sc(sv, ws, WS_CLR + wg * CLR_PER_WG + min(i * 1024 + lane * 16, CLR_PER_WG - 16));
      waitv<0>();
      TIE(ep);
      ep += 0x9E3779B97F4A7C15ull;
      const unsigned elo = (unsigned)ep, ehi = (unsigned)(ep >> 32);
      const v4u ev = {elo, ehi, elo, ehi};
      if (lane < NREP) gstore4sc(ev, ws, WS_FLAG + lane * 4096 + wg * 16);
      STAMP(1);
      for (unsigned spin = 0; spin < SPIN_CAP; ++spin) {
        __builtin_amdgcn_s_sleep(16);
        v4u a[4];
#pragma unroll
        for (int i = 0; i < 4; ++i) a[i] = __builtin_amdgcn_raw_buffer_load_b128(rs, WS_FLAG + (wg & (NREP - 1)) * 4096 + i * 1024 + lane * 16, 0, AUX_SC1);
        cfence();
        unsigned d = 0;
#pragma unroll
        for (int i = 0; i < 4; ++i) d |= (a[i].x ^ elo) | (a[i].y ^ ehi) | (a[i].z ^ elo) | (a[i].w ^ ehi);
        TIE(d);
        if (__all(d == 0u)) break;
      }
      sig(prog, 12, 1u, lane);
      STAMP(3);
    }
    if (isDma) {
      __builtin_amdgcn_s_sleep(8);
      const unsigned char* b = (const unsigned char*)ckv + ((size_t)ab * SK + asplit * 128) * 1024;
      const unsigned lb = (unsigned)(uintptr_t)(L + L_C);
      bool deep = false;
#pragma unroll
      for (int i = 0; i < 32; ++i) {
        const int r = 4 * i + (wv - 8);
        const int sw = ((r & 3) << 2) | ((r >> 2) & 3);
        if (i >= DMA_D0) {
          if (!deep) deep = __builtin_amdgcn_readfirstlane(*(volatile LAS unsigned*)(prog + 14)) >= DMA_SW;
          if (!deep) waitv<DMA_D0 - 1>(); else if (i >= DMA_DEPTH) waitv<DMA_DEPTH - 1>();
        }
        dma16(b + r * 1024, (unsigned)(((lane & 48) | ((lane & 15) ^ sw)) * 16), lb + r * 1024);
      }
      sig(prog, wv, 1u, lane);
      waitv<0>();
      sig(prog, wv, 2u, lane);
      STAMP(3);
    }
    for (;;) {
      v4u a = *(volatile LAS v4u*)(prog);
      if (__builtin_amdgcn_readfirstlane(min(min(a.x, a.y), min(a.z, a.w))) >= 4u) break;
      __builtin_amdgcn_s_sleep(2);
    }
    cfence();
    v4u wuvr[2], wor[3], wdr[4], wgur[6];
    const int td = tid - 256, wd = wv - 4;
    const int orp = min(lane / 9, 6), oc9 = lane % 9;
    const int gr = (wd >> 1) * 48 + (lane >> 3);
    const int dg = min(td / 72, 6), oc = td % 72;
    {
      const unsigned char* b = (const unsigned char*)wuv + (size_t)(64 * cs + 8 * wd) * 8192 + h * 256;
      gload4nt(wuvr[0], b, rg * 8192 + ch * 16);
      PACE();
      gload4nt(wuvr[1], b + 4 * 8192, rg * 8192 + ch * 16);
      const unsigned char* wob = (const unsigned char*)wo + (size_t)(h * 128 + 16 * wd) * 1152 + cs * 144;
      PACE();
      gload4nt(wor[0], wob, orp * 1152 + oc9 * 16);
      PACE();
      gload4nt(wor[1], wob, (7 + orp) * 1152 + oc9 * 16);
      PACE();
      gload4nt(wor[2], wob, min(14 + orp, 15) * 1152 + oc9 * 16);
    }
    const unsigned char* gup = (const unsigned char*)((wd & 1) ? wup : wgt) + (size_t)(192 * kt) * 9216 + cb * 128;
    if (ffn) {
#pragma unroll
      for (int i = 0; i < NB1; ++i) gload4nt(wgur[i], gup + (size_t)i * 8 * 9216, gr * 9216 + (lane & 7) * 16);
      waitv<NB1>();
    } else {
      waitv<0>();
    }
    STAMP(4);
    for (;;) {
      v4u a = *(volatile LAS v4u*)(prog);
      if (__builtin_amdgcn_readfirstlane(min(min(a.x, a.y), min(a.z, a.w))) >= 7u) break;
      __builtin_amdgcn_s_sleep(2);
    }
    cfence();
    TIE(wuvr[0]); TIE(wuvr[1]); TIE(wor[0]); TIE(wor[1]); TIE(wor[2]);
    *(LAS v4u*)(L + I_UV + (8 * wd + rg) * 256 + ch * 16) = wuvr[0];
    *(LAS v4u*)(L + I_UV + (8 * wd + 4 + rg) * 256 + ch * 16) = wuvr[1];
    if (lane < 63) {
      *(LAS v4u*)(L + I_WO + (16 * wd + orp) * 144 + oc9 * 16) = wor[0];
      *(LAS v4u*)(L + I_WO + (16 * wd + 7 + orp) * 144 + oc9 * 16) = wor[1];
      if (orp < 2) *(LAS v4u*)(L + I_WO + (16 * wd + 14 + orp) * 144 + oc9 * 16) = wor[2];
    }
    sig(prog, wv, 3u, lane);
    if (ffn) {
      for (;;) {
        unsigned a = *(volatile LAS unsigned*)(prog + 13);
        if (__builtin_amdgcn_readfirstlane(a) >= 1u) break;
        __builtin_amdgcn_s_sleep(2);
      }
      cfence();
#pragma unroll
      for (int i = NB1; i < 6; ++i) gload4nt(wgur[i], gup + (size_t)i * 8 * 9216, gr * 9216 + (lane & 7) * 16);
      const unsigned char* b3 = (const unsigned char*)wdn + (size_t)(cb * 64 + kt * 24) * 1152;
#pragma unroll
      for (int i = 0; i < 4; ++i) gload4nt(wdr[i], b3, min(dg + 7 * i, nd - 1) * 1152 + oc * 16);
      waitv<4>();
#pragma unroll
      for (int i = 0; i < 6; ++i) { TIE(wgur[i]); *(LAS v4u*)(L + I_GU + (gr + 8 * i) * 272 + (wd & 1) * 128 + (lane & 7) * 16) = wgur[i]; }
      waitv<0>();
      if (td < 504) {
#pragma unroll
        for (int i = 0; i < 4; ++i) { TIE(wdr[i]); *(LAS v4u*)(L + I_DN + (dg + 7 * i) * 1168 + oc * 16) = dg + 7 * i < nd ? wdr[i] : v4u{0u, 0u, 0u, 0u}; }
        if (dg < 4) *(LAS v4u*)(L + I_DN + (28 + dg) * 1168 + oc * 16) = v4u{0u, 0u, 0u, 0u};
      }
      sig(prog, wv, 4u, lane);
    }
    STAMP(6);
    STAMP_FLUSH();
    return;
  }

  TIE(ep);
  const unsigned elo = __builtin_amdgcn_readfirstlane((unsigned)ep), ehi = __builtin_amdgcn_readfirstlane((unsigned)(ep >> 32));
  const unsigned tag = elo ^ 0x5bd1e995u, saltA = ehi + elo * 0x85EBCA6Bu, saltB = (elo ^ ehi) * 0xC2B2AE35u + 0x27D4EB2Fu;
  if (wv == 0) {
    wait4(prog, 0, 1u);
    f32x4 s = *(LAS f32x4*)(L + L_QP + lane * 16);
#pragma unroll
    for (int w = 1; w < 4; ++w) s += *(LAS f32x4*)(L + L_QP + w * 1024 + lane * 16);
    {
      const v4u su = __builtin_bit_cast(v4u, s);
      __builtin_amdgcn_raw_buffer_store_b128(su, rs, WS_Q0 + (h * 8 + cs) * 1024 + lane * 16, 0, AUX_SC1);
      const v4u tr = {tag, wave_xor(xor4(su)) ^ saltA, wave_add(add4(su)) + saltB, tag};
      __builtin_amdgcn_raw_buffer_store_b128(tr, rs, lane == 0 ? WS_T0 + (h * 8 + cs) * 128 : OOB, 0, AUX_SC1);
    }
    STAMP(3);
    v4u pv[8];
    for (unsigned spin = 0; spin < SPIN_CAP; ++spin) {
      const v4u tr = __builtin_amdgcn_raw_buffer_load_b128(rs, WS_T0 + (h * 8 + (lane & 7)) * 128, 0, AUX_SC1);
#pragma unroll
      for (int i = 0; i < 8; ++i) pv[i] = __builtin_amdgcn_raw_buffer_load_b128(rs, WS_Q0 + (h * 8 + i) * 1024 + lane * 16, 0, AUX_SC1);
      cfence();
      if (__all(tr.x == tag && tr.w == tag)) {
        unsigned vx[8], va[8];
#pragma unroll
        for (int i = 0; i < 8; ++i) { vx[i] = xor4(pv[i]); va[i] = add4(pv[i]); }
        unsigned cx = tred8<false>(vx, lane), ca = tred8<true>(va, lane);
        cx ^= dppu<ROR8>(cx); ca += dppu<ROR8>(ca);
        { auto t = __builtin_amdgcn_permlane16_swap(cx, cx, false, false); cx = t[0] ^ t[1]; }
        { auto t = __builtin_amdgcn_permlane16_swap(ca, ca, false, false); ca = t[0] + t[1]; }
        { auto t = __builtin_amdgcn_permlane32_swap(cx, cx, false, false); cx = t[0] ^ t[1]; }
        { auto t = __builtin_amdgcn_permlane32_swap(ca, ca, false, false); ca = t[0] + t[1]; }
        if (__all((cx ^ saltA) == tr.y && ca + saltB == tr.z)) break;
      }
      __builtin_amdgcn_s_sleep(SL0);
    }
    f32x4 qs = __builtin_bit_cast(f32x4, pv[0]);
#pragma unroll
    for (int i = 1; i < 8; ++i) qs += __builtin_bit_cast(f32x4, pv[i]);
    *(LAS f32x4*)(L + L_QV + lane * 16) = qs;
    sig(prog, 14, 1u, lane);
    STAMP(16);
  } else {
    wait1(prog, 14, 1u);
  }
  {
    LAS unsigned char* qv = L + L_QV;
    const f32x4 q00 = *(LAS f32x4*)(qv + (ch * 8) * 4), q01 = *(LAS f32x4*)(qv + (ch * 8 + 4) * 4);
    const f32x4 q10 = *(LAS f32x4*)(qv + (128 + ch * 8) * 4), q11 = *(LAS f32x4*)(qv + (128 + ch * 8 + 4) * 4);
    float d0[4], d1[4];
    waitv<0>();
#pragma unroll
    for (int i = 0; i < 4; ++i) {
      TIE(wukv[i]);
      const v4u w = wukv[i];
      float s0 = blo(w[0]) * q00[0], s1 = blo(w[0]) * q10[0];
      s0 = fmaf(bhi(w[0]), q00[1], s0); s1 = fmaf(bhi(w[0]), q10[1], s1);
      s0 = fmaf(blo(w[1]), q00[2], s0); s1 = fmaf(blo(w[1]), q10[2], s1);
      s0 = fmaf(bhi(w[1]), q00[3], s0); s1 = fmaf(bhi(w[1]), q10[3], s1);
      s0 = fmaf(blo(w[2]), q01[0], s0); s1 = fmaf(blo(w[2]), q11[0], s1);
      s0 = fmaf(bhi(w[2]), q01[1], s0); s1 = fmaf(bhi(w[2]), q11[1], s1);
      s0 = fmaf(blo(w[3]), q01[2], s0); s1 = fmaf(blo(w[3]), q11[2], s1);
      s0 = fmaf(bhi(w[3]), q01[3], s0); s1 = fmaf(bhi(w[3]), q11[3], s1);
      d0[i] = sum_row16(s0); d1[i] = sum_row16(s1);
    }
    constexpr float QS = 0.08838834764831845f * 1.4426950408889634f;
    const bool sel = ch & 1;
    v2u o;
    o.x = cvtpk((sel ? d1[0] : d0[0]) * QS, (sel ? d1[1] : d0[1]) * QS);
    o.y = cvtpk((sel ? d1[2] : d0[2]) * QS, (sel ? d1[3] : d0[3]) * QS);
    if (ch < 2) *(LAS v2u*)(L + L_QO + (ch & 1) * 128 + (16 * wv + 4 * rg) * 2) = o;
  }
  sig(prog, wv, 3u, lane);
  if (wv == 0) {
    wait4(prog, 0, 3u);
    const v4u o = *(LAS v4u*)(L + L_QO + (lane & 15) * 16);
    __builtin_amdgcn_raw_buffer_store_b128(o, rs, lane < 16 ? WS_QA + (lane >> 3) * 32768 + h * 1024 + cs * 128 + (lane & 7) * 16 : OOB, 0, AUX_SC1);
    unsigned cx = xor4(o), ca = add4(o);
    cx ^= dppu<XOR1>(cx); cx ^= dppu<XOR2>(cx); cx ^= dppu<HMIR>(cx);
    ca += dppu<XOR1>(ca); ca += dppu<XOR2>(ca); ca += dppu<HMIR>(ca);
    const v4u tr = {tag, cx ^ saltA, ca + saltB, tag};
    __builtin_amdgcn_raw_buffer_store_b128(tr, rs, (lane & 7) < 2 ? WS_T1 + ((lane >> 4) + 4 * (lane & 1)) * 8192 + ((lane >> 3) & 1) * 4096 + (h * 8 + cs) * 16 : OOB, 0, AUX_SC1);
    STAMP(4);
    sig(prog, 14, 2u, lane);
  } else {
    wait1(prog, 14, 2u);
  }
  STAMP(24);
  wait4(prog, 8, 1u);
  STAMP(6);
  {
    const int base = WS_QA + ab * 32768;
    v4u qf[8];
    for (unsigned spin = 0; spin < SPIN_CAP; ++spin) {
      const v4u tr = __builtin_amdgcn_raw_buffer_load_b128(rs, WS_T1 + (wg & (NREP - 1)) * 8192 + ab * 4096 + ((8 * wv + (lane & 7)) * 8 + (lane >> 3)) * 16, 0, AUX_SC1);
#pragma unroll
      for (int i = 0; i < 8; ++i) qf[i] = __builtin_amdgcn_raw_buffer_load_b128(rs, base + (8 * wv + i) * 1024 + lane * 16, 0, AUX_SC1);
      cfence();
      if (__all(tr.x == tag && tr.w == tag)) {
        unsigned vx[8], va[8];
#pragma unroll
        for (int i = 0; i < 8; ++i) { vx[i] = xor4(qf[i]); va[i] = add4(qf[i]); }
        const unsigned cx = tred8<false>(vx, lane), ca = tred8<true>(va, lane);
        if (__all((cx ^ saltA) == tr.y && ca + saltB == tr.z)) break;
      }
      __builtin_amdgcn_s_sleep(SL1);
    }
    if (lane < 62) {
#pragma unroll
      for (int i = 0; i < 8; ++i) *(LAS v4u*)(L + L_S + (8 * wv + i) * QPITCH + ((lane ^ (wv >> 1)) * 16)) = qf[i];
    }
  }
  sig(prog, wv, 4u, lane);
  STAMP(5);
  wait4(prog, 0, 4u);
  const v4u q31 = __builtin_amdgcn_raw_buffer_load_b128(rs, WS_QA + ab * 32768 + (lane & 31) * 1024 + 992 + (lane >> 5) * 16, 0, AUX_SC1);
  wait4(prog, 8, 2u);
  STAMP(26);
  const int r31 = lane & 31, hh = lane >> 5;
  f32x16 sacc;
#pragma unroll
  for (int i = 0; i < 16; ++i) sacc[i] = 0.f;
  {
    const int row = 32 * wv + r31;
    const int sw = ((row & 3) << 2) | ((row >> 2) & 3);
    LAS unsigned char* arow = L + L_C + row * 1024;
    LAS unsigned char* brow = L + L_S + r31 * QPITCH + ((hh ^ (r31 >> 4)) * 16);
    constexpr int W = 6;
    v4u af[32], bf[32];
#pragma unroll
    for (int s = 0; s < W; ++s) {
      const int c64 = 2 * s + hh;
      af[s] = *(LAS v4u*)(arow + (c64 >> 4) * 256 + (((c64 & 15) ^ sw) * 16));
      bf[s] = *(LAS v4u*)(brow + s * 32);
    }
    __builtin_amdgcn_sched_barrier(0);
#pragma unroll
    for (int s = 0; s < 32; ++s) {
      if (s + W < 32) {
        const int c64 = 2 * (s + W) + hh;
        af[s + W] = *(LAS v4u*)(arow + (c64 >> 4) * 256 + (((c64 & 15) ^ sw) * 16));
        bf[s + W] = s + W < 31 ? *(LAS v4u*)(brow + (s + W) * 32) : q31;
      }
      sacc = __builtin_amdgcn_mfma_f32_32x32x16_bf16(__builtin_bit_cast(bf16x8, af[s]), __builtin_bit_cast(bf16x8, bf[s]), sacc, 0, 0, 0);
      __builtin_amdgcn_sched_barrier(0);
    }
  }
  float mx = sacc[0];
#pragma unroll
  for (int i = 1; i < 16; ++i) mx = fmaxf(mx, sacc[i]);
  mx = xmax32(mx);
  if (lane < 32) *(LAS float*)(L + L_MX + (wv * 32 + lane) * 4) = mx;
  sig(prog, wv, 5u, lane);
  STAMP(17);
  wait4(prog, 0, 5u);
#pragma unroll
  for (int w = 0; w < 4; ++w) mx = fmaxf(mx, *(LAS float*)(L + L_MX + (w * 32 + r31) * 4));
  {
    float ls = 0.f;
    v4u pf[2];
#pragma unroll
    for (int i = 0; i < 8; ++i) {
      const float p0 = __builtin_amdgcn_exp2f(sacc[2 * i] - mx), p1 = __builtin_amdgcn_exp2f(sacc[2 * i + 1] - mx);
      const unsigned pk = cvtpk(p0, p1);
      ls += blo(pk) + bhi(pk);
      pf[i >> 2][i & 3] = pk;
    }
    ls = xsum32(ls);
    *(LAS v4u*)(L + L_P + (wv * 2 + 0) * 1024 + lane * 16) = pf[0];
    *(LAS v4u*)(L + L_P + (wv * 2 + 1) * 1024 + lane * 16) = pf[1];
    if (lane < 32) *(LAS float*)(L + L_LS + (wv * 32 + lane) * 4) = ls;
  }
  sig(prog, wv, 6u, lane);
  wait4(prog, 0, 6u);
  STAMP(7);
  f32x16 oacc[4];
#pragma unroll
  for (int cb = 0; cb < 4; ++cb)
#pragma unroll
    for (int i = 0; i < 16; ++i) oacc[cb][i] = 0.f;
  float lt = 0.f;
  {
    const int blk = (lane >> 4) & 1, q4 = (lane & 15) >> 2, p4 = lane & 3;
    LAS unsigned char* tb = L + L_C + (4 * hh + q4) * 1024 + wv * 256 + 8 * (p4 & 1);
    v4u pb[8];
#pragma unroll
    for (int ks = 0; ks < 8; ++ks) pb[ks] = *(LAS v4u*)(L + L_P + ks * 1024 + lane * 16);
#pragma unroll
    for (int w = 0; w < 4; ++w) lt += *(LAS float*)(L + L_LS + (w * 32 + r31) * 4);
    constexpr int W = 5;
    s16x4 a[32][2];
#define TRLD(n) { _Pragma("unroll") for (int t = 0; t < 2; ++t) { \
      const int chunk = ((((n) & 3) ^ q4) << 2) | ((2 * blk + (p4 >> 1)) ^ (2 * t + hh)); \
      a[n][t] = __builtin_amdgcn_ds_read_tr16_b64_v4i16((LAS s16x4*)(tb + (16 * ((n) >> 2) + 8 * t) * 1024 + chunk * 16)); } }
#pragma unroll
    for (int n = 0; n < W; ++n) TRLD(n)
    __builtin_amdgcn_sched_barrier(0);
#pragma unroll
    for (int n = 0; n < 32; ++n) {
      if (n + W < 32) TRLD(n + W)
      const bf16x8 A = __builtin_shufflevector(a[n][0], a[n][1], 0, 1, 2, 3, 4, 5, 6, 7);
      oacc[n & 3] = __builtin_amdgcn_mfma_f32_32x32x16_bf16(A, __builtin_bit_cast(bf16x8, pb[n >> 2]), oacc[n & 3], 0, 0, 0);
      if (n == 27) sig(prog, wv, 7u, lane);
      __builtin_amdgcn_sched_barrier(0);
    }
#undef TRLD
  }
  STAMP(18);
  {
    const float inv = __builtin_amdgcn_rcpf(lt);
    LAS unsigned char* st = L + L_ST + wv * 4096;
#pragma unroll
    for (int half = 0; half < 2; ++half) {
#pragma unroll
      for (int c2 = 0; c2 < 2; ++c2)
#pragma unroll
        for (int g = 0; g < 4; ++g) {
          const f32x16& o = oacc[2 * half + c2];
          v2u pk;
          pk.x = cvtpk(o[4 * g] * inv, o[4 * g + 1] * inv); pk.y = cvtpk(o[4 * g + 2] * inv, o[4 * g + 3] * inv);
          *(LAS v2u*)(st + r31 * 128 + (((4 * c2 + g) ^ (r31 & 7)) * 16) + hh * 8) = pk;
        }
      waitl();
      v4u ov[4];
#pragma unroll
      for (int i = 0; i < 4; ++i) { const int hd = 8 * i + (lane >> 3); ov[i] = *(LAS v4u*)(st + hd * 128 + (((lane & 7) ^ (hd & 7)) * 16)); }
#pragma unroll
      for (int i = 0; i < 4; ++i) {
        const int hd = 8 * i + (lane >> 3);
        __builtin_amdgcn_raw_buffer_store_b128(ov[i], rs, WS_PART + (((hd * 8 + 2 * wv + half) * 2 + ab) * 128 + asplit) * 128 + (lane & 7) * 16, 0, AUX_SC1);
      }
      cfence();
    }
    sig(prog, wv, 8u, lane);
    STAMP(8);
    const unsigned lu = sanef(mx + __builtin_amdgcn_logf(lt));
    wait1(prog, 12, 1u);
    waitv<0>();
    if (lane < 32) __builtin_amdgcn_raw_buffer_store_b128(v4u{lu, lu, lu, lu}, rs, WS_LSE + (((lane * 4 + wv) * 2 + ab) * 128 + asplit) * 16, 0, AUX_SC1);
  }
  STAMP(9);
  const int tq = ch >> 2, tp = lane & 3;
  const int trow = 8 * rg + tq, tcol = (tp >> 1) * 16 + (tp & 1) * 8;
  const f32x4 z4 = {0.f, 0.f, 0.f, 0.f};
  wait4(prog, 4, 3u); wait4(prog, 8, 3u);
  bf16x8 auv[2][2], awo[4], awo4[4];
#pragma unroll
  for (int ks = 0; ks < 2; ++ks)
#pragma unroll
    for (int t = 0; t < 2; ++t) auv[ks][t] = wfrag(L + I_UV + (32 * ks + trow) * 256 + (2 * wv + t) * 32 + tcol, 256);
#pragma unroll
  for (int ks = 0; ks < 4; ++ks) {
    awo[ks] = wfrag(L + I_WO + (32 * ks + trow) * 144 + wv * 32 + tcol, 144);
    awo4[ks] = wfrag(L + I_WO + (32 * ks + trow) * 144 + 128 + tcol, 144);
  }
  {
    const int rbase = WS_PART + ((h * 8 + cs) * 2 + (wv >> 1)) * 128 * 128;
    const int lbase = WS_LSE + ((h * 4 + (cs >> 1)) * 2 + (wv >> 1)) * 2048 + lane * 16;
    unsigned l0, l1;
    for (unsigned spin = 0; spin < SPIN_CAP; ++spin) {
      l0 = __builtin_amdgcn_raw_buffer_load_b32(rs, lbase, 0, AUX_SC1);
      l1 = __builtin_amdgcn_raw_buffer_load_b32(rs, lbase + 1024, 0, AUX_SC1);
      cfence();
      if (__all(max(l0, l1) != SENT)) break;
      __builtin_amdgcn_s_sleep(1);
    }
    cfence();
    STAMP(10);
    v4u pv[8];
    const int pbase = rbase + 64 * (wv & 1) * 128 + lane * 16;
#pragma unroll
    for (int i = 0; i < 8; ++i) pv[i] = __builtin_amdgcn_raw_buffer_load_b128(rs, pbase + i * 1024, 0, AUX_SC1);
    const float m = xmax32(xmax16(max_row16(fmaxf(asf(l0), asf(l1)))));
    const float e0 = __builtin_amdgcn_exp2f(asf(l0) - m), e1 = __builtin_amdgcn_exp2f(asf(l1) - m);
    const float it = __builtin_amdgcn_rcpf(wave_sum(e0 + e1));
    wait4(prog, 0, 8u);
    *(LAS float*)(L + T_WG + wv * 256 + lane * 4) = ((wv & 1) ? e1 : e0) * it;
    waitl();
    float acc[8];
#pragma unroll
    for (int k = 0; k < 8; ++k) acc[k] = 0.f;
#pragma unroll
    for (int i = 0; i < 8; ++i) {
      const float w = *(LAS float*)(L + T_WG + wv * 256 + (8 * i + (lane >> 3)) * 4);
#pragma unroll
      for (int k = 0; k < 4; ++k) { acc[2 * k] = fmaf(w, blo(pv[i][k]), acc[2 * k]); acc[2 * k + 1] = fmaf(w, bhi(pv[i][k]), acc[2 * k + 1]); }
    }
    float t[4], u2[2];
#pragma unroll
    for (int k = 0; k < 4; ++k) t[k] = tr32(acc[k], acc[k + 4]);
#pragma unroll
    for (int k = 0; k < 2; ++k) u2[k] = tr16(t[k], t[k + 2]);
    u2[0] += dpp<ROR8>(u2[0]); u2[1] += dpp<ROR8>(u2[1]);
    if ((lane & 8) == 0) *(LAS f32x2*)(L + T_CC + (wv * 64 + (lane & 7) * 8 + (lane >> 5) * 4 + ((lane >> 4) & 1) * 2) * 4) = f32x2{u2[0], u2[1]};
  }
  sig(prog, wv, 9u, lane);
  STAMP(11);
  wait4(prog, 0, 9u);
  {
    f32x4 c[2][4];
#pragma unroll
    for (int ks = 0; ks < 2; ++ks) {
      LAS unsigned char* cp = L + T_CC + ((ch & 1) * 128 + 32 * ks + 8 * rg) * 4;
      c[ks][0] = *(LAS f32x4*)(cp); c[ks][1] = *(LAS f32x4*)(cp + 256); c[ks][2] = *(LAS f32x4*)(cp + 16); c[ks][3] = *(LAS f32x4*)(cp + 272);
    }
    f32x4 d[2] = {z4, z4};
#pragma unroll
    for (int ks = 0; ks < 2; ++ks) {
      v2u h0, l0, h1, l1;
      split4(c[ks][0] + c[ks][1], h0, l0); split4(c[ks][2] + c[ks][3], h1, l1);
      const v4u b = (ch & 2) ? v4u{l0.x, l0.y, l1.x, l1.y} : v4u{h0.x, h0.y, h1.x, h1.y};
#pragma unroll
      for (int t = 0; t < 2; ++t) d[t] = mfma16(auv[ks][t], b, d[t]);
    }
#pragma unroll
    for (int t = 0; t < 2; ++t) publish4(rs, ch < 2 ? WS_CX + (h * 8 + cs) * 1024 + (ch * 128 + 32 * wv + 16 * t + 4 * rg) * 4 : OOB, hilo(d[t]));
  }
  STAMP(19);
  if (wv == 0) {
    const f32x4 cx = gather8(rs, WS_CX + h * 8192, lane);
    v2u hi, lo;
    split4(cx, hi, lo);
    *(LAS v2u*)(L + T_CX + hh * 272 + r31 * 8) = hi;
    *(LAS v2u*)(L + T_CX + (2 + hh) * 272 + r31 * 8) = lo;
    sig(prog, 14, 3u, lane);
  } else {
    wait1(prog, 14, 3u);
  }
  if (wv == 3) sig(prog, 13, 1u, lane);
  STAMP(20);
  {
    v4u b[4];
#pragma unroll
    for (int ks = 0; ks < 4; ++ks) b[ks] = *(LAS v4u*)(L + T_CX + (ch & 3) * 272 + (32 * ks + 8 * rg) * 2);
    f32x4 d = z4, d4 = z4;
#pragma unroll
    for (int ks = 0; ks < 4; ++ks) { d = mfma16(awo[ks], b[ks], d); d4 = mfma16(awo4[ks], b[ks], d4); }
    const int o = ch * 576 + 72 * cs + 16 * wv + 4 * rg, o4 = ch * 576 + 72 * cs + 64 + 4 * rg;
    publish4(rs, ch < 2 ? WS_OP + ((o >> 4) * 32 + h) * 64 + (o & 15) * 4 : OOB, hilo(d));
    publish4(rs, (ch < 2 && rg < 2 && wv == 2) ? WS_OP + ((o4 >> 4) * 32 + h) * 64 + (o4 & 15) * 4 : OOB, hilo(d4));
  }
  STAMP(12);
  if (owner) {
    if (wv < 2) {
      const v2u xv = *(const v2u*)(x + own * 16 + (lane & 3) * 4);
      const f32x4 s = own_reduce<2>(rs, WS_OP + own * 2048, lane);
      const f32x4 hown = f32x4{blo(xv.x) + s[0], bhi(xv.x) + s[1], blo(xv.y) + s[2], bhi(xv.y) + s[3]};
      if (lane < 4) publish4(rs, WS_HB + own * 64 + lane * 16, hown);
      STAMP(21);
      const f32x4 s2 = own_reduce<14>(rs, WS_DP + own * (NFF * 64), lane);
      v2u o;
      o.x = cvtpk(hown[0] + s2[0], hown[1] + s2[1]);
      o.y = cvtpk(hown[2] + s2[2], hown[3] + s2[3]);
      if (lane < 4) *(v2u*)(out + own * 16 + lane * 4) = o;
      if (own == 0 && lane == 0)
        __hip_atomic_store((u64*)(ws + WS_EPOCH), ep + 1ull, __ATOMIC_RELAXED, __HIP_MEMORY_SCOPE_AGENT);
      STAMP(15);
    }
  } else if (ffn) {
    wait4(prog, 4, 4u); wait4(prog, 8, 4u);
    bf16x8 ag[6], au[6], wa[9];
    {
      LAS unsigned char* ap = L + I_GU + trow * 272 + wv * 32 + tcol;
#pragma unroll
      for (int ks = 0; ks < 6; ++ks) { ag[ks] = wfrag(ap + ks * 32 * 272, 272); au[ks] = wfrag(ap + ks * 32 * 272 + 128, 272); }
#pragma unroll
      for (int t = 0; t < 9; ++t) wa[t] = wfrag(L + I_DN + trow * 1168 + (9 * wv + t) * 32 + tcol, 1168);
    }
    if (wv < 2) {
      v4u hv[3];
      for (unsigned spin = 0; spin < SPIN_CAP; ++spin) {
#pragma unroll
        for (int k = 0; k < 2; ++k) hv[k] = __builtin_amdgcn_raw_buffer_load_b128(rs, WS_HB + wv * 2304 + k * 1024 + lane * 16, 0, AUX_SC1);
        hv[2] = __builtin_amdgcn_raw_buffer_load_b128(rs, lane < 16 ? WS_HB + wv * 2304 + 2048 + lane * 16 : OOB, 0, AUX_SC1);
        cfence();
        if (__all(max(max(max4(hv[0]), max4(hv[1])), max4(hv[2])) != SENT)) break;
      }
      STAMP(13);
      f32x4 f[3];
#pragma unroll
      for (int k = 0; k < 3; ++k) f[k] = __builtin_bit_cast(f32x4, hv[k]);
      const f32x4 t = f[0] + f[1] + f[2];
      const float mu = wave_sum((t[0] + t[1]) + (t[2] + t[3])) * (1.0f / DM);
      f[0] -= mu; f[1] -= mu; f[2] -= (lane < 16 ? mu : 0.f);
      const f32x4 q = f[0] * f[0] + f[1] * f[1] + f[2] * f[2];
      const float r = __builtin_amdgcn_rsqf(wave_sum((q[0] + q[1]) + (q[2] + q[3])) * (1.0f / DM) + 1e-5f);
#pragma unroll
      for (int k = 0; k < 3; ++k) {
        v2u hi, lo;
        split4(f[k] * r, hi, lo);
        if (k < 2 || lane < 16) {
          *(LAS v2u*)(L + T_XN + wv * 1168 + k * 512 + lane * 8) = hi;
          *(LAS v2u*)(L + T_XN + (2 + wv) * 1168 + k * 512 + lane * 8) = lo;
        }
      }
      sig(prog, 16 + wv, 1u, lane);
      STAMP(22);
    }
    wait1(prog, 16, 1u); wait1(prog, 17, 1u);
    {
      f32x4 dg_ = z4, du_ = z4;
      LAS unsigned char* bp = L + T_XN + (ch & 3) * 1168 + (192 * kt + 8 * rg) * 2;
      v4u b[6];
#pragma unroll
      for (int ks = 0; ks < 6; ++ks) b[ks] = *(LAS v4u*)(bp + ks * 64);
#pragma unroll
      for (int ks = 0; ks < 6; ++ks) { dg_ = mfma16(ag[ks], b[ks], dg_); du_ = mfma16(au[ks], b[ks], du_); }
      const int ob = ch < 2 ? WS_GX + fi * 1024 + (ch * 64 + 16 * wv + 4 * rg) * 4 : OOB;
      publish4(rs, ob, hilo(dg_));
      publish4(rs, ob + 512, hilo(du_));
    }
    STAMP(23);
    if (wv == 0) {
      v4u pv[3];
      poll_recs<3>(pv, rs, WS_GX + cb * 3072 + lane * 16);
      const f32x4 sgu = __builtin_bit_cast(f32x4, pv[0]) + __builtin_bit_cast(f32x4, pv[1]) + __builtin_bit_cast(f32x4, pv[2]);
      f32x4 a;
#pragma unroll
      for (int k = 0; k < 4; ++k) {
        auto t = __builtin_amdgcn_permlane32_swap(asu(sgu[k]), asu(sgu[k]), false, false);
        const float g = asf(t[0]), u = asf(t[1]);
        a[k] = g * __builtin_amdgcn_rcpf(1.0f + __expf(-g)) * u;
      }
      v2u hi, lo;
      split4(a, hi, lo);
      *(LAS v2u*)(L + T_ACT + (2 * hh + ((lane >> 4) & 1)) * 176 + ch * 8) = hh ? lo : hi;
      if (lane < 16) *(LAS v2u*)(L + T_ACT + (lane >> 2) * 176 + 128 + (lane & 3) * 8) = v2u{0u, 0u};
      sig(prog, 14, 5u, lane);
    } else {
      wait1(prog, 14, 5u);
    }
    STAMP(25);
    {
      const v4u b = *(LAS v4u*)(L + T_ACT + (ch & 3) * 176 + (24 * kt + 8 * rg) * 2);
      const int ob = ch < 2 ? WS_DP + ((ch * 36 + 9 * wv) * NFF + fi) * 64 + rg * 16 : OOB;
      f32x4 d[9];
#pragma unroll
      for (int t = 0; t < 9; ++t) d[t] = mfma16(wa[t], b, z4);
#pragma unroll
      for (int t = 0; t < 9; ++t) publish4(rs, ob + t * (NFF * 64), hilo(d[t]));
    }
    STAMP(14);
  }
  STAMP_FLUSH();
}
}

extern "C" void kernel_launch(void* const* d_in, const int* in_sizes, int n_in,
                              void* d_out, int out_size, void* d_ws, size_t ws_size,
                              hipStream_t stream) {
  using mk::u16;
  (void)hipFuncSetAttribute((const void*)mk::fused, hipFuncAttributeMaxDynamicSharedMemorySize, mk::L_BYTES);
  mk::fused<<<mk::NWG, mk::NT, mk::L_BYTES, stream>>>(
      (const u16*)d_in[0], (const u16*)d_in[1], (const u16*)d_in[2], (const u16*)d_in[3], (const u16*)d_in[4], (const u16*)d_in[5],
      (const u16*)d_in[7], (const u16*)d_in[8], (const u16*)d_in[9], (u16*)d_out, (unsigned char*)d_ws);
}
```

```cpp
#include <hip/hip_runtime.h>
#include <hip/hip_bf16.h>
#include <stdint.h>

#define MK_STAMPS 0
#define DMA_DEPTH 16

#define GLOAD_WQ gload4nt
#define SL0 4
#define SL1 4
#define TAIL_AT 0
#define TAIL_PACE 63
#define PACE() waitv<TAIL_PACE - 1>()
#define NB1 2
#define DMA_D0 2
#define DMA_SW 1u
#define B2_AT 2
#define NB2 5
#define SL2 4
#define SLP 2
#define PRE_HB 0
#define PRE_H4 64
#define PRE_G 16
#define SLH 4
namespace mk {
constexpr int SK = 16384, DM = 576;
constexpr int NWG = 256, NT = 768;
constexpr int NOWN = 72;
constexpr int NFF = 216;

constexpr int WS_EPOCH = 0;
constexpr int WS_FLAG = 4096, NREP = 8;
constexpr int WS_QA = 65536;
constexpr int WS_Q0 = WS_QA + 65536;
constexpr int WS_T0 = WS_Q0 + 262144;
constexpr int WS_T1 = WS_T0 + 32768;
constexpr int WS_CLR = WS_T1 + NREP * 8192;
constexpr int WS_CX = WS_CLR;
constexpr int WS_OP = WS_CX + 262144;
constexpr int WS_DP = WS_OP + 2 * 32 * 2304;
constexpr int GXREC = 1664;
constexpr int WS_GX = WS_DP + NOWN * NFF * 64;
constexpr int WS_LSE = WS_GX + NFF * GXREC + 1024;
constexpr int WS_CLR_END = WS_LSE + 32 * 4 * 256 * 16;
constexpr int CLR_PER_WG = (WS_CLR_END - WS_CLR) / NWG;
static_assert(CLR_PER_WG * NWG == WS_CLR_END - WS_CLR && CLR_PER_WG % 16 == 0 && CLR_PER_WG <= 9 * 1024, "clear split");
constexpr int WS_PART = 4 << 20;
constexpr int WS_STAMP = 24 << 20;
constexpr int WS_BYTES = 32 << 20;

constexpr int L_C = 0;
constexpr int L_S = 131072;
constexpr int L_CTRL = 162816;
constexpr int L_BYTES = 163840;
constexpr int L_XL = L_S;
constexpr int L_QP = L_S + 5120;
constexpr int L_QV = L_S + 13312;
constexpr int L_QO = L_S + 14336;
constexpr int QPITCH = 992;
constexpr int L_P = L_S;
constexpr int L_LS = L_S + 8192;
constexpr int L_MX = L_CTRL + 128;
constexpr int I_UV = 0;
constexpr int I_WO = 16384;
constexpr int I_GU = 34944;
constexpr int I_DN = 88064;
constexpr int L_ST = 145536;
constexpr int T_WG = L_ST;
constexpr int T_CC = L_ST + 1024;
constexpr int T_CX = L_ST + 2048;
constexpr int T_XN = L_ST + 4096;
constexpr int T_HP = L_ST + 7424;
constexpr int T_ACT = L_ST + 9216;
static_assert(I_GU + 192 * 272 <= I_DN && I_DN + 32 * 1168 <= L_S && L_ST + 16384 <= L_CTRL, "LDS map");

#define LAS __attribute__((address_space(3)))
typedef unsigned v4u __attribute__((ext_vector_type(4)));
typedef unsigned v2u __attribute__((ext_vector_type(2)));
typedef float f32x4 __attribute__((ext_vector_type(4)));
typedef float f32x2 __attribute__((ext_vector_type(2)));
typedef float f32x16 __attribute__((ext_vector_type(16)));
typedef short bf16x8 __attribute__((ext_vector_type(8)));
typedef short s16x4 __attribute__((ext_vector_type(4)));
typedef unsigned short u16;
typedef unsigned long long u64;
#define DI __device__ __forceinline__
constexpr int AUX_SC1 = 16;
constexpr unsigned SENT = 0xFFFFFFFFu;
constexpr unsigned SPIN_CAP = 1u << 16;

DI float asf(unsigned u) { return __builtin_bit_cast(float, u); }
DI unsigned asu(float f) { return __builtin_bit_cast(unsigned, f); }
DI float blo(unsigned u) { return asf(u << 16); }
DI float bhi(unsigned u) { return asf(u & 0xffff0000u); }
DI unsigned cvtpk(float lo, float hi) { unsigned r; asm("v_cvt_pk_bf16_f32 %0, %1, %2" : "=v"(r) : "v"(lo), "v"(hi)); return r; }
DI unsigned sane(unsigned u) { return u == SENT ? 0x7FC07FC0u : u; }
DI unsigned sanef(float f) { unsigned u = asu(f); return u == SENT ? 0x7FC00000u : u; }
template <int CTRL> DI float dpp(float x) { return asf((unsigned)__builtin_amdgcn_mov_dpp((int)asu(x), CTRL, 0xf, 0xf, true)); }
constexpr int XOR1 = 0xB1, XOR2 = 0x4E, HMIR = 0x141, MIR = 0x140, ROR4 = 0x124, ROR8 = 0x128;
DI float sum_row16(float x) { x += dpp<XOR1>(x); x += dpp<XOR2>(x); x += dpp<HMIR>(x); x += dpp<MIR>(x); return x; }
DI float max_row16(float x) { x = fmaxf(x, dpp<XOR1>(x)); x = fmaxf(x, dpp<XOR2>(x)); x = fmaxf(x, dpp<HMIR>(x)); x = fmaxf(x, dpp<MIR>(x)); return x; }
DI float xsum16(float x) { auto s = __builtin_amdgcn_permlane16_swap(asu(x), asu(x), false, false); return asf(s[0]) + asf(s[1]); }
DI float xsum32(float x) { auto s = __builtin_amdgcn_permlane32_swap(asu(x), asu(x), false, false); return asf(s[0]) + asf(s[1]); }
DI float xmax16(float x) { auto s = __builtin_amdgcn_permlane16_swap(asu(x), asu(x), false, false); return fmaxf(asf(s[0]), asf(s[1])); }
DI float xmax32(float x) { auto s = __builtin_amdgcn_permlane32_swap(asu(x), asu(x), false, false); return fmaxf(asf(s[0]), asf(s[1])); }
DI float tr32(float a, float b) { auto s = __builtin_amdgcn_permlane32_swap(asu(a), asu(b), false, false); return asf(s[0]) + asf(s[1]); }
DI float tr16(float a, float b) { auto s = __builtin_amdgcn_permlane16_swap(asu(a), asu(b), false, false); return asf(s[0]) + asf(s[1]); }
template <int CTRL> DI unsigned dppu(unsigned x) { return (unsigned)__builtin_amdgcn_mov_dpp((int)x, CTRL, 0xf, 0xf, true); }
DI unsigned xor4(const v4u& a) { return (a.x ^ a.y) ^ (a.z ^ a.w); }
DI unsigned add4(const v4u& a) { return (a.x + a.y) + (a.z + a.w); }
DI unsigned wave_xor(unsigned x) {
  x ^= dppu<XOR1>(x); x ^= dppu<XOR2>(x); x ^= dppu<HMIR>(x); x ^= dppu<MIR>(x);
  auto s = __builtin_amdgcn_permlane16_swap(x, x, false, false); x = s[0] ^ s[1];
  auto t = __builtin_amdgcn_permlane32_swap(x, x, false, false); return t[0] ^ t[1];
}
DI unsigned wave_add(unsigned x) {
  x += dppu<XOR1>(x); x += dppu<XOR2>(x); x += dppu<HMIR>(x); x += dppu<MIR>(x);
  auto s = __builtin_amdgcn_permlane16_swap(x, x, false, false); x = s[0] + s[1];
  auto t = __builtin_amdgcn_permlane32_swap(x, x, false, false); return t[0] + t[1];
}
template <bool ADD> DI unsigned tred8(const unsigned (&v)[8], int lane) {
  const bool b2 = lane & 4, b1 = lane & 2, b0 = lane & 1;
  unsigned a[4], b[2];
#pragma unroll
  for (int k = 0; k < 4; ++k) { const unsigned keep = b2 ? v[k + 4] : v[k], send = b2 ? v[k] : v[k + 4], r = dppu<HMIR>(send); a[k] = ADD ? keep + r : keep ^ r; }
#pragma unroll
  for (int k = 0; k < 2; ++k) { const unsigned keep = b1 ? a[k + 2] : a[k], send = b1 ? a[k] : a[k + 2], r = dppu<XOR2>(send); b[k] = ADD ? keep + r : keep ^ r; }
  const unsigned keep = b0 ? b[1] : b[0], send = b0 ? b[0] : b[1], r = dppu<XOR1>(send);
  return ADD ? keep + r : keep ^ r;
}
DI float wave_sum(float x) { return xsum32(xsum16(sum_row16(x))); }

template <int N> DI void waitv() { asm volatile("s_waitcnt vmcnt(%0)" ::"i"(N) : "memory"); }
DI void waitl() { asm volatile("s_waitcnt lgkmcnt(0)" ::: "memory"); }
DI void cfence() { asm volatile("" ::: "memory"); }
DI void bar() { asm volatile("s_waitcnt lgkmcnt(0)\n\ts_barrier" ::: "memory"); }
#define TIE(x) asm volatile("" : "+v"(x))
#define TIE8(a) asm volatile("" : "+v"(a[0]), "+v"(a[1]), "+v"(a[2]), "+v"(a[3]), "+v"(a[4]), "+v"(a[5]), "+v"(a[6]), "+v"(a[7]))

DI void gload4(v4u& d, const void* sb, unsigned vo) { asm volatile("global_load_dwordx4 %0, %1, %2" : "=v"(d) : "v"(vo), "s"(sb) : "memory"); }
DI void gload4nt(v4u& d, const void* sb, unsigned vo) { asm volatile("global_load_dwordx4 %0, %1, %2 nt" : "=v"(d) : "v"(vo), "s"(sb) : "memory"); }
DI void gload1p(unsigned& d, const void* p) { asm volatile("global_load_dword %0, %1, off nt" : "=v"(d) : "v"(p) : "memory"); }
DI void gloadh(unsigned& d, const void* sb, unsigned vo) { asm volatile("global_load_ushort %0, %1, %2" : "=v"(d) : "v"(vo), "s"(sb) : "memory"); }
DI void gload2sc(u64& d, const void* sb, unsigned vo) { asm volatile("global_load_dwordx2 %0, %1, %2 sc1" : "=v"(d) : "v"(vo), "s"(sb) : "memory"); }
DI void gstore4sc(const v4u& d, void* sb, unsigned vo) { asm volatile("global_store_dwordx4 %0, %1, %2 sc1\n\ts_nop 1" ::"v"(vo), "v"(d), "s"(sb) : "memory"); }
DI void gstore2sc(const u64& d, void* sb, unsigned vo) { asm volatile("global_store_dwordx2 %0, %1, %2 sc1\n\ts_nop 1" ::"v"(vo), "v"(d), "s"(sb) : "memory"); }
DI void dma16(const void* sb, unsigned vo, unsigned ldsaddr) {
  unsigned keep;
  asm volatile("s_mov_b32 %0, m0\n\ts_mov_b32 m0, %3\n\ts_nop 0\n\tglobal_load_lds_dwordx4 %1, %2 nt\n\ts_mov_b32 m0, %0"
               : "=&s"(keep) : "v"(vo), "s"(sb), "s"(ldsaddr) : "memory");
}

#define STAMP(k) do { } while (0)
#define STAMP_FLUSH() do { } while (0)
#define STAMP_DECL

DI void sig(LAS unsigned* prog, int idx, unsigned v, int lane) {
  waitl();
  if (lane == 0) *(volatile LAS unsigned*)(prog + idx) = v;
  cfence();
}
DI void wait4(LAS unsigned* prog, int first, unsigned v) {
  for (;;) {
    v4u a = *(volatile LAS v4u*)(prog + first);
    unsigned mn = min(min(a.x, a.y), min(a.z, a.w));
    if (__builtin_amdgcn_readfirstlane(mn) >= v) break;
  }
  cfence();
}
DI void wait1(LAS unsigned* prog, int idx, unsigned v) {
  for (;;) {
    unsigned a = *(volatile LAS unsigned*)(prog + idx);
    if (__builtin_amdgcn_readfirstlane(a) >= v) break;
  }
  cfence();
}
DI unsigned max4(const v4u& a) { return max(max(a.x, a.y), max(a.z, a.w)); }

DI void fma8(float (&a0)[8], float (&a1)[8], const v4u& w, float x0, float x1) {
#pragma unroll
  for (int k = 0; k < 4; ++k) {
    const float lo = blo(w[k]), hi = bhi(w[k]);
    a0[2 * k] = fmaf(lo, x0, a0[2 * k]); a1[2 * k] = fmaf(lo, x1, a1[2 * k]);
    a0[2 * k + 1] = fmaf(hi, x0, a0[2 * k + 1]); a1[2 * k + 1] = fmaf(hi, x1, a1[2 * k + 1]);
  }
}

constexpr int OOB = 0x70000000;
template <int N> DI void poll_recs(v4u (&pv)[N], __amdgpu_buffer_rsrc_t rs, int base, bool hiHalf = false) {
  for (unsigned spin = 0; spin < SPIN_CAP; ++spin) {
#pragma unroll
    for (int i = 0; i < N; ++i) pv[i] = __builtin_amdgcn_raw_buffer_load_b128(rs, (N == 14 && i == 13 && hiHalf) ? OOB : base + i * 1024, 0, AUX_SC1);
    cfence();
    unsigned m = max4(pv[0]);
#pragma unroll
    for (int i = 1; i < N; ++i) m = max(m, max4(pv[i]));
    if (__all(m != SENT)) break;
    __builtin_amdgcn_s_sleep(SLP);
  }
}
template <int N> DI f32x4 own_reduce(__amdgpu_buffer_rsrc_t rs, int base, int lane) {
  v4u pv[N];
  poll_recs<N>(pv, rs, base + lane * 16, lane >= 32);
  f32x4 s = __builtin_bit_cast(f32x4, pv[0]);
#pragma unroll
  for (int i = 1; i < N; ++i) s += __builtin_bit_cast(f32x4, pv[i]);
#pragma unroll
  for (int k = 0; k < 4; ++k) { float t = s[k]; t += dpp<ROR8>(t); t += dpp<ROR4>(t); s[k] = xsum32(xsum16(t)); }
  return s;
}
DI f32x4 gather8(__amdgpu_buffer_rsrc_t rs, int base, int lane) {
  v4u pv[8];
  poll_recs<8>(pv, rs, base + lane * 16);
  f32x4 s = __builtin_bit_cast(f32x4, pv[0]);
#pragma unroll
  for (int i = 1; i < 8; ++i) s += __builtin_bit_cast(f32x4, pv[i]);
  return s;
}
DI void publish4(__amdgpu_buffer_rsrc_t rs, int off, const f32x4& s) {
  const v4u o = {sanef(s[0]), sanef(s[1]), sanef(s[2]), sanef(s[3])};
  __builtin_amdgcn_raw_buffer_store_b128(o, rs, off, 0, AUX_SC1);
}
DI void split4(const f32x4& v, v2u& hi, v2u& lo) {
  hi.x = cvtpk(v[0], v[1]); hi.y = cvtpk(v[2], v[3]);
  lo.x = cvtpk(v[0] - blo(hi.x), v[1] - bhi(hi.x)); lo.y = cvtpk(v[2] - blo(hi.y), v[3] - bhi(hi.y));
}
DI bf16x8 wfrag(LAS unsigned char* p, int pitch) {
  const s16x4 a = __builtin_amdgcn_ds_read_tr16_b64_v4i16((LAS s16x4*)p);
  const s16x4 b = __builtin_amdgcn_ds_read_tr16_b64_v4i16((LAS s16x4*)(p + 4 * pitch));
  return __builtin_shufflevector(a, b, 0, 1, 2, 3, 4, 5, 6, 7);
}
DI f32x4 mfma16(const bf16x8& a, const v4u& b, const f32x4& c) {
  return __builtin_amdgcn_mfma_f32_16x16x32_bf16(a, __builtin_bit_cast(bf16x8, b), c, 0, 0, 0);
}
DI f32x4 hilo(const f32x4& d) { f32x4 r; _Pragma("unroll") for (int k = 0; k < 4; ++k) r[k] = d[k] + dpp<XOR2>(d[k]); return r; }

__global__ void __launch_bounds__(NT, 3)
fused(const u16* __restrict__ x, const u16* __restrict__ ckv, const u16* __restrict__ wq, const u16* __restrict__ wuk,
      const u16* __restrict__ wuv, const u16* __restrict__ wo, const u16* __restrict__ wgt, const u16* __restrict__ wup,
      const u16* __restrict__ wdn, u16* __restrict__ out, unsigned char* __restrict__ ws) {
  extern __shared__ __attribute__((aligned(16))) unsigned char smem[];
  LAS unsigned char* L = (LAS unsigned char*)smem;
  LAS unsigned* prog = (LAS unsigned*)(L + L_CTRL);
  const int tid = threadIdx.x, lane = tid & 63, wv = __builtin_amdgcn_readfirstlane(tid >> 6);
  const int wg = blockIdx.x, xcd = wg & 7, jj = wg >> 3;
  const int h = xcd * 4 + (jj >> 3), cs = jj & 7;
  const int ab = wg >> 7, asplit = wg & 127;
  const bool ffn = jj < 27;
  const int cb = xcd * 9 + min(jj / 3, 8), kt = jj % 3, fi = cb * 3 + kt;
  const int nd = kt < 2 ? 24 : 16;
  const bool owner = jj >= 27 && (jj - 27) * 8 + xcd < 36;
  const int own = ((jj - 27) * 8 + xcd) * 2 + (wv & 1);
  const int rg = lane >> 4, ch = lane & 15;
  const __amdgpu_buffer_rsrc_t rs = __builtin_amdgcn_make_buffer_rsrc((void*)ws, 0, WS_BYTES, 0x00020000);
  const bool isC = wv < 4, isAdm = wv == 4, isDma = wv >= 8;
  STAMP_DECL;
  STAMP(0);
  if (tid < 32) prog[tid] = 0u;
  bar();

  u64 ep = 0;
  float xmean0 = 0.f, xmean1 = 0.f;
  v4u xa, xb, xc, wqv[5], wukv[4]; unsigned xo0, xo1;
  if (isAdm) {
    gload2sc(ep, ws, WS_EPOCH);
  } else if (isC) {
    const int m0 = 72 * cs + 18 * wv;
    gload2sc(ep, ws, WS_EPOCH);
    gload4(xa, x, lane * 16);
    gload4(xb, x, 1024 + lane * 16);
    gload4(xc, x, 2048 + (lane & 15) * 16);
    gloadh(xo0, x, (m0 + min(lane, 17)) * 2);
    gloadh(xo1, x, 1152 + (m0 + min(lane, 17)) * 2);
    const unsigned char* b = (const unsigned char*)wq + (size_t)m0 * 8192 + h * 256;
#pragma unroll
    for (int i = 0; i < 4; ++i) gload4nt(wqv[i], b + i * 4 * 8192, rg * 8192 + ch * 16);
    gload4nt(wqv[4], b, (16 + (rg & 1)) * 8192 + ch * 16);
    const unsigned char* b2 = (const unsigned char*)wuk + (size_t)(64 * cs + 16 * wv) * 8192 + h * 256;
#pragma unroll
    for (int i = 0; i < 4; ++i) gload4nt(wukv[i], b2 + i * 8192, (4 * rg) * 8192 + ch * 16);
  }
  if (isC) {
    waitv<9>();
    TIE(xa); TIE(xb); TIE(xc); TIE(xo0); TIE(xo1);
    STAMP(1);
    float sa = 0.f, sb = 0.f, sc = 0.f;
#pragma unroll
    for (int k = 0; k < 4; ++k) { sa += blo(xa[k]) + bhi(xa[k]); sb += blo(xb[k]) + bhi(xb[k]); sc += blo(xc[k]) + bhi(xc[k]); }
    const bool b0 = lane < 8, c1 = lane < 16;
    const float m0 = wave_sum(sa + (b0 ? sb : 0.f)) * (1.0f / DM);
    const float m1 = wave_sum((b0 ? 0.f : sb) + (c1 ? sc : 0.f)) * (1.0f / DM);
    const float mb = b0 ? m0 : m1;
    xmean0 = m0; xmean1 = m1;
    float va = 0.f, vb = 0.f, vc = 0.f;
#pragma unroll
    for (int k = 0; k < 4; ++k) {
      float d;
      d = blo(xa[k]) - m0; va += d * d; d = bhi(xa[k]) - m0; va += d * d;
      d = blo(xb[k]) - mb; vb += d * d; d = bhi(xb[k]) - mb; vb += d * d;
      d = blo(xc[k]) - m1; vc += d * d; d = bhi(xc[k]) - m1; vc += d * d;
    }
    const float v0 = wave_sum(va + (b0 ? vb : 0.f)) * (1.0f / DM);
    const float v1 = wave_sum((b0 ? 0.f : vb) + (c1 ? vc : 0.f)) * (1.0f / DM);
    const float r0 = __builtin_amdgcn_rsqf(v0 + 1e-5f), r1 = __builtin_amdgcn_rsqf(v1 + 1e-5f);
    if (lane < 20) *(LAS f32x2*)(L + L_XL + (wv * 20 + lane) * 8) = lane < 18 ? f32x2{(blo(xo0) - m0) * r0, (blo(xo1) - m1) * r1} : f32x2{0.f, 0.f};
    waitl();
    f32x2 xr[5];
#pragma unroll
    for (int i = 0; i < 5; ++i) xr[i] = *(LAS f32x2*)(L + L_XL + (wv * 20 + 4 * i + rg) * 8);
    float a0[8], a1[8];
#pragma unroll
    for (int k = 0; k < 8; ++k) { a0[k] = 0.f; a1[k] = 0.f; }
    waitv<4>();
#pragma unroll
    for (int i = 0; i < 5; ++i) { TIE(wqv[i]); fma8(a0, a1, wqv[i], xr[i][0], xr[i][1]); }
    float t[8];
#pragma unroll
    for (int k = 0; k < 8; ++k) t[k] = tr32(a0[k], a1[k]);
    f32x4 o;
#pragma unroll
    for (int k = 0; k < 4; ++k) o[k] = tr16(t[k], t[k + 4]);
    *(LAS f32x4*)(L + L_QP + (wv * 256 + (lane >> 5) * 128 + ch * 8 + (rg & 1) * 4) * 4) = o;
    sig(prog, wv, 1u, lane);
    STAMP(2);
  }

  if (!isC) {
    if (isAdm) {
      const v4u sv = {SENT, SENT, SENT, SENT};
#pragma unroll
      for (int i = 0; i < 9; ++i) gstore4sc(sv, ws, WS_CLR + wg * CLR_PER_WG + min(i * 1024 + lane * 16, CLR_PER_WG - 16));
      waitv<0>();
      TIE(ep);
      ep += 0x9E3779B97F4A7C15ull;
      const unsigned elo = (unsigned)ep, ehi = (unsigned)(ep >> 32);
      const v4u ev = {elo, ehi, elo, ehi};
      if (lane < NREP) gstore4sc(ev, ws, WS_FLAG + lane * 4096 + wg * 16);
      STAMP(1);
      for (unsigned spin = 0; spin < SPIN_CAP; ++spin) {
        __builtin_amdgcn_s_sleep(16);
        v4u a[4];
#pragma unroll
        for (int i = 0; i < 4; ++i) a[i] = __builtin_amdgcn_raw_buffer_load_b128(rs, WS_FLAG + (wg & (NREP - 1)) * 4096 + i * 1024 + lane * 16, 0, AUX_SC1);
        cfence();
        unsigned d = 0;
#pragma unroll
        for (int i = 0; i < 4; ++i) d |= (a[i].x ^ elo) | (a[i].y ^ ehi) | (a[i].z ^ elo) | (a[i].w ^ ehi);
        TIE(d);
        if (__all(d == 0u)) break;
      }
      sig(prog, 12, 1u, lane);
      STAMP(3);
    }
    if (isDma) {
      __builtin_amdgcn_s_sleep(8);
      const unsigned char* b = (const unsigned char*)ckv + ((size_t)ab * SK + asplit * 128) * 1024;
      const unsigned lb = (unsigned)(uintptr_t)(L + L_C);
      bool deep = false;
#pragma unroll
      for (int i = 0; i < 32; ++i) {
        const int r = 4 * i + (wv - 8);
        const int sw = ((r & 3) << 2) | ((r >> 2) & 3);
        if (i >= DMA_D0) {
          if (!deep) deep = __builtin_amdgcn_readfirstlane(*(volatile LAS unsigned*)(prog + 14)) >= DMA_SW;
          if (!deep) waitv<DMA_D0 - 1>(); else if (i >= DMA_DEPTH) waitv<DMA_DEPTH - 1>();
        }
        dma16(b + r * 1024, (unsigned)(((lane & 48) | ((lane & 15) ^ sw)) * 16), lb + r * 1024);
      }
      sig(prog, wv, 1u, lane);
      waitv<0>();
      sig(prog, wv, 2u, lane);
      STAMP(3);
    }
    for (;;) {
      v4u a = *(volatile LAS v4u*)(prog);
      if (__builtin_amdgcn_readfirstlane(min(min(a.x, a.y), min(a.z, a.w))) >= 4u) break;
      __builtin_amdgcn_s_sleep(2);
    }
    cfence();
    v4u wuvr[2], wor[3], wdr[4], wgur[6];
    const int td = tid - 256, wd = wv - 4;
    const int orp = min(lane / 9, 6), oc9 = lane % 9;
    const int gr = (wd >> 1) * 48 + (lane >> 3);
    const int dg = min(td / 72, 6), oc = td % 72;
    {
      const unsigned char* b = (const unsigned char*)wuv + (size_t)(64 * cs + 8 * wd) * 8192 + h * 256;
      gload4nt(wuvr[0], b, rg * 8192 + ch * 16);
      PACE();
      gload4nt(wuvr[1], b + 4 * 8192, rg * 8192 + ch * 16);
      const unsigned char* wob = (const unsigned char*)wo + (size_t)(h * 128 + 16 * wd) * 1152 + cs * 144;
      PACE();
      gload4nt(wor[0], wob, orp * 1152 + oc9 * 16);
      PACE();
      gload4nt(wor[1], wob, (7 + orp) * 1152 + oc9 * 16);
      PACE();
      gload4nt(wor[2], wob, min(14 + orp, 15) * 1152 + oc9 * 16);
    }
    const unsigned char* gup = (const unsigned char*)((wd & 1) ? wup : wgt) + (size_t)(192 * kt) * 9216 + cb * 128;
    if (ffn) {
#pragma unroll
      for (int i = 0; i < NB1; ++i) gload4nt(wgur[i], gup + (size_t)i * 8 * 9216, gr * 9216 + (lane & 7) * 16);
      waitv<NB1>();
    } else {
      waitv<0>();
    }
    STAMP(4);
    for (;;) {
      v4u a = *(volatile LAS v4u*)(prog);
      if (__builtin_amdgcn_readfirstlane(min(min(a.x, a.y), min(a.z, a.w))) >= 7u) break;
      __builtin_amdgcn_s_sleep(2);
    }
    cfence();
    TIE(wuvr[0]); TIE(wuvr[1]); TIE(wor[0]); TIE(wor[1]); TIE(wor[2]);
    *(LAS v4u*)(L + I_UV + (8 * wd + rg) * 256 + ch * 16) = wuvr[0];
    *(LAS v4u*)(L + I_UV + (8 * wd + 4 + rg) * 256 + ch * 16) = wuvr[1];
    if (lane < 63) {
      *(LAS v4u*)(L + I_WO + (16 * wd + orp) * 144 + oc9 * 16) = wor[0];
      *(LAS v4u*)(L + I_WO + (16 * wd + 7 + orp) * 144 + oc9 * 16) = wor[1];
      if (orp < 2) *(LAS v4u*)(L + I_WO + (16 * wd + 14 + orp) * 144 + oc9 * 16) = wor[2];
    }
    sig(prog, wv, 3u, lane);
    if (ffn) {
      for (;;) {
        unsigned a = *(volatile LAS unsigned*)(prog + 13);
        if (__builtin_amdgcn_readfirstlane(a) >= 1u) break;
        __builtin_amdgcn_s_sleep(2);
      }
      cfence();
#pragma unroll
      for (int i = NB1; i < NB2; ++i) gload4nt(wgur[i], gup + (size_t)i * 8 * 9216, gr * 9216 + (lane & 7) * 16);
      for (;;) {
        unsigned a = *(volatile LAS unsigned*)(prog + 13);
        if (__builtin_amdgcn_readfirstlane(a) >= 2u) break;
        __builtin_amdgcn_s_sleep(2);
      }
      cfence();
#pragma unroll
      for (int i = NB2; i < 6; ++i) gload4nt(wgur[i], gup + (size_t)i * 8 * 9216, gr * 9216 + (lane & 7) * 16);
      const unsigned char* b3 = (const unsigned char*)wdn + (size_t)(cb * 64 + kt * 24) * 1152;
#pragma unroll
      for (int i = 0; i < 4; ++i) gload4nt(wdr[i], b3, min(dg + 7 * i, nd - 1) * 1152 + oc * 16);
      waitv<4>();
#pragma unroll
      for (int i = 0; i < 6; ++i) { TIE(wgur[i]); *(LAS v4u*)(L + I_GU + (gr + 8 * i) * 272 + (wd & 1) * 128 + (lane & 7) * 16) = wgur[i]; }
      sig(prog, wv, 4u, lane);
      waitv<0>();
      if (td < 504) {
#pragma unroll
        for (int i = 0; i < 4; ++i) { TIE(wdr[i]); *(LAS v4u*)(L + I_DN + (dg + 7 * i) * 1168 + oc * 16) = dg + 7 * i < nd ? wdr[i] : v4u{0u, 0u, 0u, 0u}; }
        if (dg < 4) *(LAS v4u*)(L + I_DN + (28 + dg) * 1168 + oc * 16) = v4u{0u, 0u, 0u, 0u};
      }
      sig(prog, wv, 5u, lane);
    }
    STAMP(6);
    STAMP_FLUSH();
    return;
  }

  TIE(ep);
  const unsigned elo = __builtin_amdgcn_readfirstlane((unsigned)ep), ehi = __builtin_amdgcn_readfirstlane((unsigned)(ep >> 32));
  const unsigned tag = elo ^ 0x5bd1e995u, saltA = ehi + elo * 0x85EBCA6Bu, saltB = (elo ^ ehi) * 0xC2B2AE35u + 0x27D4EB2Fu;
  if (wv == 0) {
    wait4(prog, 0, 1u);
    f32x4 s = *(LAS f32x4*)(L + L_QP + lane * 16);
#pragma unroll
    for (int w = 1; w < 4; ++w) s += *(LAS f32x4*)(L + L_QP + w * 1024 + lane * 16);
    {
      const v4u su = __builtin_bit_cast(v4u, s);
      __builtin_amdgcn_raw_buffer_store_b128(su, rs, WS_Q0 + (h * 8 + cs) * 1024 + lane * 16, 0, AUX_SC1);
      const v4u tr = {tag, wave_xor(xor4(su)) ^ saltA, wave_add(add4(su)) + saltB, tag};
      __builtin_amdgcn_raw_buffer_store_b128(tr, rs, lane == 0 ? WS_T0 + (h * 8 + cs) * 128 : OOB, 0, AUX_SC1);
    }
    STAMP(3);
    v4u pv[8];
    for (unsigned spin = 0; spin < SPIN_CAP; ++spin) {
      const v4u tr = __builtin_amdgcn_raw_buffer_load_b128(rs, WS_T0 + (h * 8 + (lane & 7)) * 128, 0, AUX_SC1);
#pragma unroll
      for (int i = 0; i < 8; ++i) pv[i] = __builtin_amdgcn_raw_buffer_load_b128(rs, WS_Q0 + (h * 8 + i) * 1024 + lane * 16, 0, AUX_SC1);
      cfence();
      if (__all(tr.x == tag && tr.w == tag)) {
        unsigned vx[8], va[8];
#pragma unroll
        for (int i = 0; i < 8; ++i) { vx[i] = xor4(pv[i]); va[i] = add4(pv[i]); }
        unsigned cx = tred8<false>(vx, lane), ca = tred8<true>(va, lane);
        cx ^= dppu<ROR8>(cx); ca += dppu<ROR8>(ca);
        { auto t = __builtin_amdgcn_permlane16_swap(cx, cx, false, false); cx = t[0] ^ t[1]; }
        { auto t = __builtin_amdgcn_permlane16_swap(ca, ca, false, false); ca = t[0] + t[1]; }
        { auto t = __builtin_amdgcn_permlane32_swap(cx, cx, false, false); cx = t[0] ^ t[1]; }
        { auto t = __builtin_amdgcn_permlane32_swap(ca, ca, false, false); ca = t[0] + t[1]; }
        if (__all((cx ^ saltA) == tr.y && ca + saltB == tr.z)) break;
      }
      __builtin_amdgcn_s_sleep(SL0);
    }
    f32x4 qs = __builtin_bit_cast(f32x4, pv[0]);
#pragma unroll
    for (int i = 1; i < 8; ++i) qs += __builtin_bit_cast(f32x4, pv[i]);
    *(LAS f32x4*)(L + L_QV + lane * 16) = qs;
    sig(prog, 14, 1u, lane);
    STAMP(16);
  } else {
    wait1(prog, 14, 1u);
  }
  {
    LAS unsigned char* qv = L + L_QV;
    const f32x4 q00 = *(LAS f32x4*)(qv + (ch * 8) * 4), q01 = *(LAS f32x4*)(qv + (ch * 8 + 4) * 4);
    const f32x4 q10 = *(LAS f32x4*)(qv + (128 + ch * 8) * 4), q11 = *(LAS f32x4*)(qv + (128 + ch * 8 + 4) * 4);
    float d0[4], d1[4];
    waitv<0>();
#pragma unroll
    for (int i = 0; i < 4; ++i) {
      TIE(wukv[i]);
      const v4u w = wukv[i];
      float s0 = blo(w[0]) * q00[0], s1 = blo(w[0]) * q10[0];
      s0 = fmaf(bhi(w[0]), q00[1], s0); s1 = fmaf(bhi(w[0]), q10[1], s1);
      s0 = fmaf(blo(w[1]), q00[2], s0); s1 = fmaf(blo(w[1]), q10[2], s1);
      s0 = fmaf(bhi(w[1]), q00[3], s0); s1 = fmaf(bhi(w[1]), q10[3], s1);
      s0 = fmaf(blo(w[2]), q01[0], s0); s1 = fmaf(blo(w[2]), q11[0], s1);
      s0 = fmaf(bhi(w[2]), q01[1], s0); s1 = fmaf(bhi(w[2]), q11[1], s1);
      s0 = fmaf(blo(w[3]), q01[2], s0); s1 = fmaf(blo(w[3]), q11[2], s1);
      s0 = fmaf(bhi(w[3]), q01[3], s0); s1 = fmaf(bhi(w[3]), q11[3], s1);
      d0[i] = sum_row16(s0); d1[i] = sum_row16(s1);
    }
    constexpr float QS = 0.08838834764831845f * 1.4426950408889634f;
    const bool sel = ch & 1;
    v2u o;
    o.x = cvtpk((sel ? d1[0] : d0[0]) * QS, (sel ? d1[1] : d0[1]) * QS);
    o.y = cvtpk((sel ? d1[2] : d0[2]) * QS, (sel ? d1[3] : d0[3]) * QS);
    if (ch < 2) *(LAS v2u*)(L + L_QO + (ch & 1) * 128 + (16 * wv + 4 * rg) * 2) = o;
  }
  sig(prog, wv, 3u, lane);
  if (wv == 0) {
    wait4(prog, 0, 3u);
    const v4u o = *(LAS v4u*)(L + L_QO + (lane & 15) * 16);
    __builtin_amdgcn_raw_buffer_store_b128(o, rs, lane < 16 ? WS_QA + (lane >> 3) * 32768 + h * 1024 + cs * 128 + (lane & 7) * 16 : OOB, 0, AUX_SC1);
    unsigned cx = xor4(o), ca = add4(o);
    cx ^= dppu<XOR1>(cx); cx ^= dppu<XOR2>(cx); cx ^= dppu<HMIR>(cx);
    ca += dppu<XOR1>(ca); ca += dppu<XOR2>(ca); ca += dppu<HMIR>(ca);
    const v4u tr = {tag, cx ^ saltA, ca + saltB, tag};
    __builtin_amdgcn_raw_buffer_store_b128(tr, rs, (lane & 7) < 2 ? WS_T1 + ((lane >> 4) + 4 * (lane & 1)) * 8192 + ((lane >> 3) & 1) * 4096 + (h * 8 + cs) * 16 : OOB, 0, AUX_SC1);
    STAMP(4);
    sig(prog, 14, 2u, lane);
  } else {
    wait1(prog, 14, 2u);
  }
  STAMP(24);
  wait4(prog, 8, 1u);
  STAMP(6);
  {
    const int base = WS_QA + ab * 32768;
    v4u qf[8];
    for (unsigned spin = 0; spin < SPIN_CAP; ++spin) {
      const v4u tr = __builtin_amdgcn_raw_buffer_load_b128(rs, WS_T1 + (wg & (NREP - 1)) * 8192 + ab * 4096 + ((8 * wv + (lane & 7)) * 8 + (lane >> 3)) * 16, 0, AUX_SC1);
#pragma unroll
      for (int i = 0; i < 8; ++i) qf[i] = __builtin_amdgcn_raw_buffer_load_b128(rs, base + (8 * wv + i) * 1024 + lane * 16, 0, AUX_SC1);
      cfence();
      if (__all(tr.x == tag && tr.w == tag)) {
        unsigned vx[8], va[8];
#pragma unroll
        for (int i = 0; i < 8; ++i) { vx[i] = xor4(qf[i]); va[i] = add4(qf[i]); }
        const unsigned cx = tred8<false>(vx, lane), ca = tred8<true>(va, lane);
        if (__all((cx ^ saltA) == tr.y && ca + saltB == tr.z)) break;
      }
      __builtin_amdgcn_s_sleep(SL1);
    }
    if (lane < 62) {
#pragma unroll
      for (int i = 0; i < 8; ++i) *(LAS v4u*)(L + L_S + (8 * wv + i) * QPITCH + ((lane ^ (wv >> 1)) * 16)) = qf[i];
    }
  }
  sig(prog, wv, 4u, lane);
  STAMP(5);
  wait4(prog, 0, 4u);
  const v4u q31 = __builtin_amdgcn_raw_buffer_load_b128(rs, WS_QA + ab * 32768 + (lane & 31) * 1024 + 992 + (lane >> 5) * 16, 0, AUX_SC1);
  wait4(prog, 8, 2u);
  STAMP(26);
  const int r31 = lane & 31, hh = lane >> 5;
  f32x16 sacc;
#pragma unroll
  for (int i = 0; i < 16; ++i) sacc[i] = 0.f;
  {
    const int row = 32 * wv + r31;
    const int sw = ((row & 3) << 2) | ((row >> 2) & 3);
    LAS unsigned char* arow = L + L_C + row * 1024;
    LAS unsigned char* brow = L + L_S + r31 * QPITCH + ((hh ^ (r31 >> 4)) * 16);
    constexpr int W = 6;
    v4u af[32], bf[32];
#pragma unroll
    for (int s = 0; s < W; ++s) {
      const int c64 = 2 * s + hh;
      af[s] = *(LAS v4u*)(arow + (c64 >> 4) * 256 + (((c64 & 15) ^ sw) * 16));
      bf[s] = *(LAS v4u*)(brow + s * 32);
    }
    __builtin_amdgcn_sched_barrier(0);
#pragma unroll
    for (int s = 0; s < 32; ++s) {
      if (s + W < 32) {
        const int c64 = 2 * (s + W) + hh;
        af[s + W] = *(LAS v4u*)(arow + (c64 >> 4) * 256 + (((c64 & 15) ^ sw) * 16));
        bf[s + W] = s + W < 31 ? *(LAS v4u*)(brow + (s + W) * 32) : q31;
      }
      sacc = __builtin_amdgcn_mfma_f32_32x32x16_bf16(__builtin_bit_cast(bf16x8, af[s]), __builtin_bit_cast(bf16x8, bf[s]), sacc, 0, 0, 0);
      __builtin_amdgcn_sched_barrier(0);
    }
  }
  float mx = sacc[0];
#pragma unroll
  for (int i = 1; i < 16; ++i) mx = fmaxf(mx, sacc[i]);
  mx = xmax32(mx);
  if (lane < 32) *(LAS float*)(L + L_MX + (wv * 32 + lane) * 4) = mx;
  sig(prog, wv, 5u, lane);
  STAMP(17);
  wait4(prog, 0, 5u);
#pragma unroll
  for (int w = 0; w < 4; ++w) mx = fmaxf(mx, *(LAS float*)(L + L_MX + (w * 32 + r31) * 4));
  {
    float ls = 0.f;
    v4u pf[2];
#pragma unroll
    for (int i = 0; i < 8; ++i) {
      const float p0 = __builtin_amdgcn_exp2f(sacc[2 * i] - mx), p1 = __builtin_amdgcn_exp2f(sacc[2 * i + 1] - mx);
      const unsigned pk = cvtpk(p0, p1);
      ls += blo(pk) + bhi(pk);
      pf[i >> 2][i & 3] = pk;
    }
    ls = xsum32(ls);
    *(LAS v4u*)(L + L_P + (wv * 2 + 0) * 1024 + lane * 16) = pf[0];
    *(LAS v4u*)(L + L_P + (wv * 2 + 1) * 1024 + lane * 16) = pf[1];
    if (lane < 32) *(LAS float*)(L + L_LS + (wv * 32 + lane) * 4) = ls;
  }
  sig(prog, wv, 6u, lane);
  wait4(prog, 0, 6u);
  STAMP(7);
  float lt = 0.f;
  {
    f32x16 oacc[4];
#pragma unroll
    for (int cb = 0; cb < 4; ++cb)
#pragma unroll
      for (int i = 0; i < 16; ++i) oacc[cb][i] = 0.f;
    const int blk = (lane >> 4) & 1, q4 = (lane & 15) >> 2, p4 = lane & 3;
    LAS unsigned char* tb = L + L_C + (4 * hh + q4) * 1024 + wv * 256 + 8 * (p4 & 1);
    v4u pb[8];
#pragma unroll
    for (int ks = 0; ks < 8; ++ks) pb[ks] = *(LAS v4u*)(L + L_P + ks * 1024 + lane * 16);
#pragma unroll
    for (int w = 0; w < 4; ++w) lt += *(LAS float*)(L + L_LS + (w * 32 + r31) * 4);
    const float inv = __builtin_amdgcn_rcpf(lt);
    LAS unsigned char* st = L + L_ST + wv * 4096;
    LAS unsigned char* stw = st + r31 * 128 + hh * 8;
    LAS unsigned char* str_ = st + (lane >> 3) * 128 + (((lane & 7) ^ (lane >> 3)) * 16);
    const int pofs = WS_PART + ((((lane >> 3) * 8 + 2 * wv) * 2 + ab) * 128 + asplit) * 128 + (lane & 7) * 16;
    constexpr int W = 5;
    s16x4 a[32][2];
    v4u ov[4];
#define CBK(n) (2 * ((n) >> 4) + ((n) & 1))
#define KSK(n) (((n) >> 1) & 7)
#define TRLD(n) { _Pragma("unroll") for (int t = 0; t < 2; ++t) { \
      const int chunk = ((CBK(n) ^ q4) << 2) | ((2 * blk + (p4 >> 1)) ^ (2 * t + hh)); \
      a[n][t] = __builtin_amdgcn_ds_read_tr16_b64_v4i16((LAS s16x4*)(tb + (16 * KSK(n) + 8 * t) * 1024 + chunk * 16)); } }
#define STG(half, k) { const f32x16& o = oacc[2 * (half) + ((k) >> 2)]; const int g = (k) & 3; v2u pk; \
      pk.x = cvtpk(o[4 * g] * inv, o[4 * g + 1] * inv); pk.y = cvtpk(o[4 * g + 2] * inv, o[4 * g + 3] * inv); \
      *(LAS v2u*)(stw + (((k) ^ (r31 & 7)) * 16)) = pk; }
#pragma unroll
    for (int n = 0; n < W; ++n) TRLD(n)
    __builtin_amdgcn_sched_barrier(0);
#pragma unroll
    for (int n = 0; n < 32; ++n) {
      if (n + W < 32) TRLD(n + W)
      const bf16x8 A = __builtin_shufflevector(a[n][0], a[n][1], 0, 1, 2, 3, 4, 5, 6, 7);
      oacc[CBK(n)] = __builtin_amdgcn_mfma_f32_32x32x16_bf16(A, __builtin_bit_cast(bf16x8, pb[KSK(n)]), oacc[CBK(n)], 0, 0, 0);
      if (n >= 18 && n < 26) STG(0, n - 18)
      if (n == 27) {
        sig(prog, wv, 7u, lane);
#pragma unroll
        for (int i = 0; i < 4; ++i) ov[i] = *(LAS v4u*)(str_ + i * 1024);
      }
      if (n == 30) {
#pragma unroll
        for (int i = 0; i < 4; ++i) __builtin_amdgcn_raw_buffer_store_b128(ov[i], rs, pofs + i * (8 * 8 * 2 * 128 * 128), 0, AUX_SC1);
      }
      __builtin_amdgcn_sched_barrier(0);
    }
    STAMP(18);
#pragma unroll
    for (int k = 0; k < 8; ++k) STG(1, k)
    waitl();
#pragma unroll
    for (int i = 0; i < 4; ++i) ov[i] = *(LAS v4u*)(str_ + i * 1024);
#pragma unroll
    for (int i = 0; i < 4; ++i) __builtin_amdgcn_raw_buffer_store_b128(ov[i], rs, pofs + i * (8 * 8 * 2 * 128 * 128) + 2 * 128 * 128, 0, AUX_SC1);
    cfence();
#undef TRLD
#undef STG
#undef CBK
#undef KSK
    sig(prog, wv, 8u, lane);
    STAMP(8);
    const unsigned lu = sanef(mx + __builtin_amdgcn_logf(lt));
    wait1(prog, 12, 1u);
    waitv<0>();
    if (lane < 32) __builtin_amdgcn_raw_buffer_store_b128(v4u{lu, lu, lu, lu}, rs, WS_LSE + (((lane * 4 + wv) * 2 + ab) * 128 + asplit) * 16, 0, AUX_SC1);
  }
  STAMP(9);
  const int tq = ch >> 2, tp = lane & 3;
  const int trow = 8 * rg + tq, tcol = (tp >> 1) * 16 + (tp & 1) * 8;
  const f32x4 z4 = {0.f, 0.f, 0.f, 0.f};
  wait4(prog, 4, 3u); wait4(prog, 8, 3u);
  bf16x8 auv[2][2], awo[4], awo4[4];
#pragma unroll
  for (int ks = 0; ks < 2; ++ks)
#pragma unroll
    for (int t = 0; t < 2; ++t) auv[ks][t] = wfrag(L + I_UV + (32 * ks + trow) * 256 + (2 * wv + t) * 32 + tcol, 256);
#pragma unroll
  for (int ks = 0; ks < 4; ++ks) {
    awo[ks] = wfrag(L + I_WO + (32 * ks + trow) * 144 + wv * 32 + tcol, 144);
    awo4[ks] = wfrag(L + I_WO + (32 * ks + trow) * 144 + 128 + tcol, 144);
  }
  {
    const int rbase = WS_PART + ((h * 8 + cs) * 2 + (wv >> 1)) * 128 * 128;
    const int lbase = WS_LSE + ((h * 4 + (cs >> 1)) * 2 + (wv >> 1)) * 2048 + lane * 16;
    v4u pv[8];
    const int pbase = rbase + 64 * (wv & 1) * 128 + lane * 16;
    if ((wv & 1) == 0) {
      unsigned l0, l1;
      for (unsigned spin = 0; spin < SPIN_CAP; ++spin) {
        l0 = __builtin_amdgcn_raw_buffer_load_b32(rs, lbase, 0, AUX_SC1);
        l1 = __builtin_amdgcn_raw_buffer_load_b32(rs, lbase + 1024, 0, AUX_SC1);
        cfence();
        if (__all(max(l0, l1) != SENT)) break;
        __builtin_amdgcn_s_sleep(SL2);
      }
      sig(prog, 18 + (wv >> 1), 1u, lane);
#pragma unroll
      for (int i = 0; i < 8; ++i) pv[i] = __builtin_amdgcn_raw_buffer_load_b128(rs, pbase + i * 1024, 0, AUX_SC1);
      const float m = xmax32(xmax16(max_row16(fmaxf(asf(l0), asf(l1)))));
      const float e0 = __builtin_amdgcn_exp2f(asf(l0) - m), e1 = __builtin_amdgcn_exp2f(asf(l1) - m);
      const float it = __builtin_amdgcn_rcpf(wave_sum(e0 + e1));
      wait4(prog, 0, 8u);
      *(LAS float*)(L + T_WG + wv * 256 + lane * 4) = e0 * it;
      *(LAS float*)(L + T_WG + wv * 256 + 256 + lane * 4) = e1 * it;
      sig(prog, 18 + (wv >> 1), 2u, lane);
    } else {
      wait1(prog, 18 + (wv >> 1), 1u);
#pragma unroll
      for (int i = 0; i < 8; ++i) pv[i] = __builtin_amdgcn_raw_buffer_load_b128(rs, pbase + i * 1024, 0, AUX_SC1);
      wait1(prog, 18 + (wv >> 1), 2u);
    }
    STAMP(10);
    float acc[8];
#pragma unroll
    for (int k = 0; k < 8; ++k) acc[k] = 0.f;
#pragma unroll
    for (int i = 0; i < 8; ++i) {
      const float w = *(LAS float*)(L + T_WG + wv * 256 + (8 * i + (lane >> 3)) * 4);
#pragma unroll
      for (int k = 0; k < 4; ++k) { acc[2 * k] = fmaf(w, blo(pv[i][k]), acc[2 * k]); acc[2 * k + 1] = fmaf(w, bhi(pv[i][k]), acc[2 * k + 1]); }
    }
    float t[4], u2[2];
#pragma unroll
    for (int k = 0; k < 4; ++k) t[k] = tr32(acc[k], acc[k + 4]);
#pragma unroll
    for (int k = 0; k < 2; ++k) u2[k] = tr16(t[k], t[k + 2]);
    u2[0] += dpp<ROR8>(u2[0]); u2[1] += dpp<ROR8>(u2[1]);
    if ((lane & 8) == 0) *(LAS f32x2*)(L + T_CC + (wv * 64 + (lane & 7) * 8 + (lane >> 5) * 4 + ((lane >> 4) & 1) * 2) * 4) = f32x2{u2[0], u2[1]};
  }
  sig(prog, wv, 9u, lane);
  STAMP(11);
  wait4(prog, 0, 9u);
  {
    f32x4 c[2][4];
#pragma unroll
    for (int ks = 0; ks < 2; ++ks) {
      LAS unsigned char* cp = L + T_CC + ((ch & 1) * 128 + 32 * ks + 8 * rg) * 4;
      c[ks][0] = *(LAS f32x4*)(cp); c[ks][1] = *(LAS f32x4*)(cp + 256); c[ks][2] = *(LAS f32x4*)(cp + 16); c[ks][3] = *(LAS f32x4*)(cp + 272);
    }
    f32x4 d[2] = {z4, z4};
#pragma unroll
    for (int ks = 0; ks < 2; ++ks) {
      v2u h0, l0, h1, l1;
      split4(c[ks][0] + c[ks][1], h0, l0); split4(c[ks][2] + c[ks][3], h1, l1);
      const v4u b = (ch & 2) ? v4u{l0.x, l0.y, l1.x, l1.y} : v4u{h0.x, h0.y, h1.x, h1.y};
#pragma unroll
      for (int t = 0; t < 2; ++t) d[t] = mfma16(auv[ks][t], b, d[t]);
    }
#pragma unroll
    for (int t = 0; t < 2; ++t) publish4(rs, ch < 2 ? WS_CX + (h * 8 + cs) * 1024 + (ch * 128 + 32 * wv + 16 * t + 4 * rg) * 4 : OOB, hilo(d[t]));
  }
  if (wv == 0) sig(prog, 13, 1u, lane);
  STAMP(19);
  if (wv == 0) {
    __builtin_amdgcn_s_sleep(PRE_G);
    const f32x4 cx = gather8(rs, WS_CX + h * 8192, lane);
    v2u hi, lo;
    split4(cx, hi, lo);
    *(LAS v2u*)(L + T_CX + hh * 272 + r31 * 8) = hi;
    *(LAS v2u*)(L + T_CX + (2 + hh) * 272 + r31 * 8) = lo;
    sig(prog, 14, 3u, lane);
  } else {
    wait1(prog, 14, 3u);
  }
  STAMP(20);
  {
    v4u b[4];
#pragma unroll
    for (int ks = 0; ks < 4; ++ks) b[ks] = *(LAS v4u*)(L + T_CX + (ch & 3) * 272 + (32 * ks + 8 * rg) * 2);
    f32x4 d = z4, d4 = z4;
#pragma unroll
    for (int ks = 0; ks < 4; ++ks) { d = mfma16(awo[ks], b[ks], d); d4 = mfma16(awo4[ks], b[ks], d4); }
    const int ob = WS_OP + (ch * 32 + h) * 2304 + (72 * cs + 4 * rg) * 4;
    publish4(rs, ch < 2 ? ob + 64 * wv : OOB, hilo(d));
    publish4(rs, (ch < 2 && rg < 2 && wv == 2) ? ob + 256 : OOB, hilo(d4));
  }
  if (wv == 0) sig(prog, 13, 2u, lane);
  STAMP(12);
  if (owner) {
    if (wv < 2) {
      const v2u xv = *(const v2u*)(x + own * 16 + (lane & 3) * 4);
      __builtin_amdgcn_s_sleep(PRE_H4);
      f32x4 s;
      {
        v4u pv[2];
        const int ob = WS_OP + ((own >= 36 ? 32 : 0) + (lane >> 2)) * 2304 + (own % 36) * 64 + (lane & 3) * 16;
        for (unsigned spin = 0; spin < SPIN_CAP; ++spin) {
          pv[0] = __builtin_amdgcn_raw_buffer_load_b128(rs, ob, 0, AUX_SC1);
          pv[1] = __builtin_amdgcn_raw_buffer_load_b128(rs, ob + 16 * 2304, 0, AUX_SC1);
          cfence();
          if (__all(max(max4(pv[0]), max4(pv[1])) != SENT)) break;
          __builtin_amdgcn_s_sleep(SLP);
        }
        s = __builtin_bit_cast(f32x4, pv[0]) + __builtin_bit_cast(f32x4, pv[1]);
#pragma unroll
        for (int k = 0; k < 4; ++k) { float t = s[k]; t += dpp<ROR8>(t); t += dpp<ROR4>(t); s[k] = xsum32(xsum16(t)); }
      }
      const f32x4 hown = f32x4{blo(xv.x) + s[0], bhi(xv.x) + s[1], blo(xv.y) + s[2], bhi(xv.y) + s[3]};
      STAMP(21);
      const f32x4 s2 = own_reduce<14>(rs, WS_DP + own * (NFF * 64), lane);
      v2u o;
      o.x = cvtpk(hown[0] + s2[0], hown[1] + s2[1]);
      o.y = cvtpk(hown[2] + s2[2], hown[3] + s2[3]);
      if (lane < 4) *(v2u*)(out + own * 16 + lane * 4) = o;
      if (own == 0 && lane == 0)
        __hip_atomic_store((u64*)(ws + WS_EPOCH), ep + 1ull, __ATOMIC_RELAXED, __HIP_MEMORY_SCOPE_AGENT);
      STAMP(15);
    }
  } else if (ffn) {
    const int fb = wv >> 1;
    const v2u xv = *(const v2u*)(x + fb * 576 + 192 * kt + min(lane, 47) * 4);
    if (wv == 3) {
      if (lane < 24) *(LAS v4u*)(L + T_XN + 4 * 400 + lane * 16) = v4u{0x3F803F80u, 0x3F803F80u, 0x3F803F80u, 0x3F803F80u};
#pragma unroll
      for (int r = 5; r < 8; ++r) if (lane < 24) *(LAS v4u*)(L + T_XN + r * 400 + lane * 16) = v4u{0u, 0u, 0u, 0u};
    }
    f32x4 hs;
    {
      v4u pv[16];
      const int ob = lane < 48 ? WS_OP + (fb * 32 + 16 * (wv & 1)) * 2304 + 768 * kt + lane * 16 : OOB;
      __builtin_amdgcn_s_sleep(PRE_G);
      for (unsigned spin = 0; spin < SPIN_CAP; ++spin) {
#pragma unroll
        for (int i = 0; i < 16; ++i) pv[i] = __builtin_amdgcn_raw_buffer_load_b128(rs, ob + i * 2304, 0, AUX_SC1);
        cfence();
        unsigned m = max4(pv[0]);
#pragma unroll
        for (int i = 1; i < 16; ++i) m = max(m, max4(pv[i]));
        if (__all(m != SENT)) break;
        __builtin_amdgcn_s_sleep(SLH);
      }
      hs = __builtin_bit_cast(f32x4, pv[0]);
#pragma unroll
      for (int i = 1; i < 16; ++i) hs += __builtin_bit_cast(f32x4, pv[i]);
    }
    STAMP(13);
    if (wv & 1) {
      if (lane < 48) *(LAS f32x4*)(L + T_HP + fb * 768 + lane * 16) = hs;
      sig(prog, 16 + fb, 1u, lane);
    } else {
      const float cm = fb ? xmean1 : xmean0;
      hs += f32x4{blo(xv.x) - cm, bhi(xv.x) - cm, blo(xv.y) - cm, bhi(xv.y) - cm};
      wait1(prog, 16 + fb, 1u);
      hs += *(LAS f32x4*)(L + T_HP + fb * 768 + min(lane, 47) * 16);
      if (lane >= 48) hs = z4;
      v2u hi, lo;
      split4(hs, hi, lo);
      if (lane < 48) {
        *(LAS v2u*)(L + T_XN + fb * 400 + lane * 8) = hi;
        *(LAS v2u*)(L + T_XN + (2 + fb) * 400 + lane * 8) = lo;
      }
      sig(prog, 16 + fb, 2u, lane);
      const float s1 = wave_sum((hs[0] + hs[1]) + (hs[2] + hs[3]));
      const float s2 = wave_sum((hs[0] * hs[0] + hs[1] * hs[1]) + (hs[2] * hs[2] + hs[3] * hs[3]));
      __builtin_amdgcn_raw_buffer_store_b64(v2u{sanef(s1), sanef(s2)}, rs, lane == 0 ? WS_GX + fi * GXREC + 1536 + fb * 8 : OOB, 0, AUX_SC1);
    }
    STAMP(22);
    wait4(prog, 4, 4u); wait4(prog, 8, 4u);
    bf16x8 wa[9];
    {
      bf16x8 ag[6], au[6];
      LAS unsigned char* ap = L + I_GU + trow * 272 + wv * 32 + tcol;
#pragma unroll
      for (int ks = 0; ks < 6; ++ks) { ag[ks] = wfrag(ap + ks * 32 * 272, 272); au[ks] = wfrag(ap + ks * 32 * 272 + 128, 272); }
      wait1(prog, 16, 2u); wait1(prog, 17, 2u);
      f32x4 dg_ = z4, du_ = z4;
      LAS unsigned char* bp = L + T_XN + (ch & 7) * 400 + 8 * rg * 2;
      v4u b[6];
#pragma unroll
      for (int ks = 0; ks < 6; ++ks) b[ks] = *(LAS v4u*)(bp + ks * 64);
#pragma unroll
      for (int ks = 0; ks < 6; ++ks) { dg_ = mfma16(ag[ks], b[ks], dg_); du_ = mfma16(au[ks], b[ks], du_); }
      const int rb = WS_GX + fi * GXREC + (16 * wv + 4 * rg) * 4;
      publish4(rs, ch < 2 ? rb + ch * 256 : (ch == 4 ? rb + 1024 : OOB), hilo(dg_));
      publish4(rs, ch < 2 ? rb + 512 + ch * 256 : (ch == 4 ? rb + 1280 : OOB), hilo(du_));
      wait4(prog, 4, 5u); wait4(prog, 8, 5u);
#pragma unroll
      for (int t = 0; t < 9; ++t) wa[t] = wfrag(L + I_DN + trow * 1168 + (9 * wv + t) * 32 + tcol, 1168);
    }
    STAMP(23);
    if (wv == 0) {
      v4u pv[3], pc[3]; v2u ps[3];
      const int gb = WS_GX + cb * 3 * GXREC;
      __builtin_amdgcn_s_sleep(PRE_G);
      for (unsigned spin = 0; spin < SPIN_CAP; ++spin) {
#pragma unroll
        for (int i = 0; i < 3; ++i) {
          pv[i] = __builtin_amdgcn_raw_buffer_load_b128(rs, gb + i * GXREC + lane * 16, 0, AUX_SC1);
          pc[i] = __builtin_amdgcn_raw_buffer_load_b128(rs, gb + i * GXREC + 1024 + hh * 256 + ch * 16, 0, AUX_SC1);
          ps[i] = __builtin_amdgcn_raw_buffer_load_b64(rs, gb + i * GXREC + 1536 + ((lane >> 4) & 1) * 8, 0, AUX_SC1);
        }
        cfence();
        unsigned m = 0;
#pragma unroll
        for (int i = 0; i < 3; ++i) m = max(max(m, max4(pv[i])), max(max4(pc[i]), max(ps[i].x, ps[i].y)));
        if (__all(m != SENT)) break;
        __builtin_amdgcn_s_sleep(SLP);
      }
      const float mu = (asf(ps[0].x) + asf(ps[1].x) + asf(ps[2].x)) * (1.0f / DM);
      const float var = fmaxf((asf(ps[0].y) + asf(ps[1].y) + asf(ps[2].y)) * (1.0f / DM) - mu * mu, 0.f);
      const float r = __builtin_amdgcn_rsqf(var + 1e-5f);
      const f32x4 csum = __builtin_bit_cast(f32x4, pc[0]) + __builtin_bit_cast(f32x4, pc[1]) + __builtin_bit_cast(f32x4, pc[2]);
      const f32x4 sgu = (__builtin_bit_cast(f32x4, pv[0]) + __builtin_bit_cast(f32x4, pv[1]) + __builtin_bit_cast(f32x4, pv[2]) - csum * mu) * r;
      f32x4 a;
#pragma unroll
      for (int k = 0; k < 4; ++k) {
        auto t = __builtin_amdgcn_permlane32_swap(asu(sgu[k]), asu(sgu[k]), false, false);
        const float g = asf(t[0]), u = asf(t[1]);
        a[k] = g * __builtin_amdgcn_rcpf(1.0f + __expf(-g)) * u;
      }
      v2u hi, lo;
      split4(a, hi, lo);
      *(LAS v2u*)(L + T_ACT + (2 * hh + ((lane >> 4) & 1)) * 176 + ch * 8) = hh ? lo : hi;
      if (lane < 16) *(LAS v2u*)(L + T_ACT + (lane >> 2) * 176 + 128 + (lane & 3) * 8) = v2u{0u, 0u};
      sig(prog, 14, 5u, lane);
    } else {
      wait1(prog, 14, 5u);
    }
    STAMP(25);
    {
      const v4u b = *(LAS v4u*)(L + T_ACT + (ch & 3) * 176 + (24 * kt + 8 * rg) * 2);
      const int ob = ch < 2 ? WS_DP + ((ch * 36 + 9 * wv) * NFF + fi) * 64 + rg * 16 : OOB;
      f32x4 d[9];
#pragma unroll
      for (int t = 0; t < 9; ++t) d[t] = mfma16(wa[t], b, z4);
#pragma unroll
      for (int t = 0; t < 9; ++t) publish4(rs, ob + t * (NFF * 64), hilo(d[t]));
    }
    STAMP(14);
  }
  STAMP_FLUSH();
}
}

extern "C" void kernel_launch(void* const* d_in, const int* in_sizes, int n_in,
                              void* d_out, int out_size, void* d_ws, size_t ws_size,
                              hipStream_t stream) {
  using mk::u16;
  (void)hipFuncSetAttribute((const void*)mk::fused, hipFuncAttributeMaxDynamicSharedMemorySize, mk::L_BYTES);
  mk::fused<<<mk::NWG, mk::NT, mk::L_BYTES, stream>>>(
      (const u16*)d_in[0], (const u16*)d_in[1], (const u16*)d_in[2], (const u16*)d_in[3], (const u16*)d_in[4], (const u16*)d_in[5],
      (const u16*)d_in[7], (const u16*)d_in[8], (const u16*)d_in[9], (u16*)d_out, (unsigned char*)d_ws);
}
```

```cpp
#include <hip/hip_runtime.h>
#include <hip/hip_bf16.h>
#include <stdint.h>

namespace mk {
constexpr int SK = 16384, DM = 576;
constexpr int NWG = 256, NT = 768;
constexpr int DMA_DEPTH = 16, DMA_D0 = 2;
constexpr int NB1 = 2, NB2 = 5;
constexpr int SL0 = 4, SL1 = 4, SL2 = 4, SLP = 2, SLH = 4, PRE_G = 16, PRE_H4 = 64;
constexpr int NOWN = 72;
constexpr int NFF = 216;

constexpr int WS_EPOCH = 0;
constexpr int WS_FLAG = 4096, NREP = 8;
constexpr int WS_QA = 65536;
constexpr int WS_Q0 = WS_QA + 65536;
constexpr int WS_T0 = WS_Q0 + 262144;
constexpr int WS_T1 = WS_T0 + 32768;
constexpr int WS_CLR = WS_T1 + NREP * 8192;
constexpr int WS_CX = WS_CLR;
constexpr int WS_OP = WS_CX + 262144;
constexpr int WS_DP = WS_OP + 2 * 32 * 2304;
constexpr int GXREC = 1664;
constexpr int WS_GX = WS_DP + NOWN * NFF * 64;
constexpr int WS_LSE = WS_GX + NFF * GXREC + 1024;
constexpr int WS_CLR_END = WS_LSE + 32 * 4 * 256 * 16;
constexpr int CLR_PER_WG = (WS_CLR_END - WS_CLR) / NWG;
static_assert(CLR_PER_WG * NWG == WS_CLR_END - WS_CLR && CLR_PER_WG % 16 == 0 && CLR_PER_WG <= 9 * 1024, "clear split");
constexpr int WS_PART = 4 << 20;
constexpr int WS_BYTES = 32 << 20;

constexpr int L_C = 0;
constexpr int L_S = 131072;
constexpr int L_CTRL = 162816;
constexpr int L_BYTES = 163840;
constexpr int L_XL = L_S;
constexpr int L_QP = L_S + 5120;
constexpr int L_QV = L_S + 13312;
constexpr int L_QO = L_S + 14336;
constexpr int QPITCH = 992;
constexpr int L_P = L_S;
constexpr int L_LS = L_S + 8192;
constexpr int L_MX = L_CTRL + 128;
constexpr int I_UV = 0;
constexpr int I_WO = 16384;
constexpr int I_GU = 34944;
constexpr int I_DN = 88064;
constexpr int L_ST = 145536;
constexpr int T_WG = L_ST;
constexpr int T_CC = L_ST + 1024;
constexpr int T_CX = L_ST + 2048;
constexpr int T_XN = L_ST + 4096;
constexpr int T_HP = L_ST + 7424;
constexpr int T_ACT = L_ST + 9216;
static_assert(I_GU + 192 * 272 <= I_DN && I_DN + 32 * 1168 <= L_S && L_ST + 16384 <= L_CTRL, "LDS map");

#define LAS __attribute__((address_space(3)))
typedef unsigned v4u __attribute__((ext_vector_type(4)));
typedef unsigned v2u __attribute__((ext_vector_type(2)));
typedef float f32x4 __attribute__((ext_vector_type(4)));
typedef float f32x2 __attribute__((ext_vector_type(2)));
typedef float f32x16 __attribute__((ext_vector_type(16)));
typedef short bf16x8 __attribute__((ext_vector_type(8)));
typedef short s16x4 __attribute__((ext_vector_type(4)));
typedef unsigned short u16;
typedef unsigned long long u64;
#define DI __device__ __forceinline__
constexpr int AUX_SC1 = 16;
constexpr unsigned SENT = 0xFFFFFFFFu;
constexpr unsigned SPIN_CAP = 1u << 16;

DI float asf(unsigned u) { return __builtin_bit_cast(float, u); }
DI unsigned asu(float f) { return __builtin_bit_cast(unsigned, f); }
DI float blo(unsigned u) { return asf(u << 16); }
DI float bhi(unsigned u) { return asf(u & 0xffff0000u); }
typedef __bf16 bf16x2v __attribute__((ext_vector_type(2)));
DI unsigned cvtpk(float lo, float hi) { return __builtin_bit_cast(unsigned, __builtin_convertvector(f32x2{lo, hi}, bf16x2v)); }
DI unsigned sane(unsigned u) { return u == SENT ? 0x7FC07FC0u : u; }
DI unsigned sanef(float f) { unsigned u = asu(f); return u == SENT ? 0x7FC00000u : u; }
template <int CTRL> DI float dpp(float x) { return asf((unsigned)__builtin_amdgcn_mov_dpp((int)asu(x), CTRL, 0xf, 0xf, true)); }
constexpr int XOR1 = 0xB1, XOR2 = 0x4E, HMIR = 0x141, MIR = 0x140, ROR4 = 0x124, ROR8 = 0x128;
DI float sum_row16(float x) { x += dpp<XOR1>(x); x += dpp<XOR2>(x); x += dpp<HMIR>(x); x += dpp<MIR>(x); return x; }
DI float max_row16(float x) { x = fmaxf(x, dpp<XOR1>(x)); x = fmaxf(x, dpp<XOR2>(x)); x = fmaxf(x, dpp<HMIR>(x)); x = fmaxf(x, dpp<MIR>(x)); return x; }
DI float xsum16(float x) { auto s = __builtin_amdgcn_permlane16_swap(asu(x), asu(x), false, false); return asf(s[0]) + asf(s[1]); }
DI float xsum32(float x) { auto s = __builtin_amdgcn_permlane32_swap(asu(x), asu(x), false, false); return asf(s[0]) + asf(s[1]); }
DI float xmax16(float x) { auto s = __builtin_amdgcn_permlane16_swap(asu(x), asu(x), false, false); return fmaxf(asf(s[0]), asf(s[1])); }
DI float xmax32(float x) { auto s = __builtin_amdgcn_permlane32_swap(asu(x), asu(x), false, false); return fmaxf(asf(s[0]), asf(s[1])); }
DI float tr32(float a, float b) { auto s = __builtin_amdgcn_permlane32_swap(asu(a), asu(b), false, false); return asf(s[0]) + asf(s[1]); }
DI float tr16(float a, float b) { auto s = __builtin_amdgcn_permlane16_swap(asu(a), asu(b), false, false); return asf(s[0]) + asf(s[1]); }
template <int CTRL> DI unsigned dppu(unsigned x) { return (unsigned)__builtin_amdgcn_mov_dpp((int)x, CTRL, 0xf, 0xf, true); }
DI unsigned xor4(const v4u& a) { return (a.x ^ a.y) ^ (a.z ^ a.w); }
DI unsigned add4(const v4u& a) { return (a.x + a.y) + (a.z + a.w); }
DI unsigned wave_xor(unsigned x) {
  x ^= dppu<XOR1>(x); x ^= dppu<XOR2>(x); x ^= dppu<HMIR>(x); x ^= dppu<MIR>(x);
  auto s = __builtin_amdgcn_permlane16_swap(x, x, false, false); x = s[0] ^ s[1];
  auto t = __builtin_amdgcn_permlane32_swap(x, x, false, false); return t[0] ^ t[1];
}
DI unsigned wave_add(unsigned x) {
  x += dppu<XOR1>(x); x += dppu<XOR2>(x); x += dppu<HMIR>(x); x += dppu<MIR>(x);
  auto s = __builtin_amdgcn_permlane16_swap(x, x, false, false); x = s[0] + s[1];
  auto t = __builtin_amdgcn_permlane32_swap(x, x, false, false); return t[0] + t[1];
}
template <bool ADD> DI unsigned tred8(const unsigned (&v)[8], int lane) {
  const bool b2 = lane & 4, b1 = lane & 2, b0 = lane & 1;
  unsigned a[4], b[2];
#pragma unroll
  for (int k = 0; k < 4; ++k) { const unsigned keep = b2 ? v[k + 4] : v[k], send = b2 ? v[k] : v[k + 4], r = dppu<HMIR>(send); a[k] = ADD ? keep + r : keep ^ r; }
#pragma unroll
  for (int k = 0; k < 2; ++k) { const unsigned keep = b1 ? a[k + 2] : a[k], send = b1 ? a[k] : a[k + 2], r = dppu<XOR2>(send); b[k] = ADD ? keep + r : keep ^ r; }
  const unsigned keep = b0 ? b[1] : b[0], send = b0 ? b[0] : b[1], r = dppu<XOR1>(send);
  return ADD ? keep + r : keep ^ r;
}
DI float wave_sum(float x) { return xsum32(xsum16(sum_row16(x))); }

template <int N> DI void waitv() { asm volatile("s_waitcnt vmcnt(%0)" ::"i"(N) : "memory"); }
DI void waitl() { asm volatile("s_waitcnt lgkmcnt(0)" ::: "memory"); }
DI void cfence() { asm volatile("" ::: "memory"); }
DI void bar() { asm volatile("s_waitcnt lgkmcnt(0)\n\ts_barrier" ::: "memory"); }
#define TIE(x) asm volatile("" : "+v"(x))
#define TIE8(a) asm volatile("" : "+v"(a[0]), "+v"(a[1]), "+v"(a[2]), "+v"(a[3]), "+v"(a[4]), "+v"(a[5]), "+v"(a[6]), "+v"(a[7]))

DI void gload4(v4u& d, const void* sb, unsigned vo) { asm volatile("global_load_dwordx4 %0, %1, %2" : "=v"(d) : "v"(vo), "s"(sb) : "memory"); }
DI void gload4nt(v4u& d, const void* sb, unsigned vo) { asm volatile("global_load_dwordx4 %0, %1, %2 nt" : "=v"(d) : "v"(vo), "s"(sb) : "memory"); }
DI void gload1p(unsigned& d, const void* p) { asm volatile("global_load_dword %0, %1, off nt" : "=v"(d) : "v"(p) : "memory"); }
DI void gloadh(unsigned& d, const void* sb, unsigned vo) { asm volatile("global_load_ushort %0, %1, %2" : "=v"(d) : "v"(vo), "s"(sb) : "memory"); }
DI void gload2sc(u64& d, const void* sb, unsigned vo) { asm volatile("global_load_dwordx2 %0, %1, %2 sc1" : "=v"(d) : "v"(vo), "s"(sb) : "memory"); }
DI void gstore4sc(const v4u& d, void* sb, unsigned vo) { asm volatile("global_store_dwordx4 %0, %1, %2 sc1\n\ts_nop 1" ::"v"(vo), "v"(d), "s"(sb) : "memory"); }
DI void gstore2sc(const u64& d, void* sb, unsigned vo) { asm volatile("global_store_dwordx2 %0, %1, %2 sc1\n\ts_nop 1" ::"v"(vo), "v"(d), "s"(sb) : "memory"); }
DI void dma16(const void* sb, unsigned vo, unsigned ldsaddr) {
  unsigned keep;
  asm volatile("s_mov_b32 %0, m0\n\ts_mov_b32 m0, %3\n\ts_nop 0\n\tglobal_load_lds_dwordx4 %1, %2 nt\n\ts_mov_b32 m0, %0"
               : "=&s"(keep) : "v"(vo), "s"(sb), "s"(ldsaddr) : "memory");
}


DI void sig(LAS unsigned* prog, int idx, unsigned v, int lane) {
  waitl();
  if (lane == 0) *(volatile LAS unsigned*)(prog + idx) = v;
  cfence();
}
DI void wait4(LAS unsigned* prog, int first, unsigned v) {
  for (;;) {
    v4u a = *(volatile LAS v4u*)(prog + first);
    unsigned mn = min(min(a.x, a.y), min(a.z, a.w));
    if (__builtin_amdgcn_readfirstlane(mn) >= v) break;
  }
  cfence();
}
DI void wait1(LAS unsigned* prog, int idx, unsigned v) {
  for (;;) {
    unsigned a = *(volatile LAS unsigned*)(prog + idx);
    if (__builtin_amdgcn_readfirstlane(a) >= v) break;
  }
  cfence();
}
DI unsigned max4(const v4u& a) { return max(max(a.x, a.y), max(a.z, a.w)); }

DI void fma8(float (&a0)[8], float (&a1)[8], const v4u& w, float x0, float x1) {
#pragma unroll
  for (int k = 0; k < 4; ++k) {
    const float lo = blo(w[k]), hi = bhi(w[k]);
    a0[2 * k] = fmaf(lo, x0, a0[2 * k]); a1[2 * k] = fmaf(lo, x1, a1[2 * k]);
    a0[2 * k + 1] = fmaf(hi, x0, a0[2 * k + 1]); a1[2 * k + 1] = fmaf(hi, x1, a1[2 * k + 1]);
  }
}

constexpr int OOB = 0x70000000;
template <int N> DI void poll_recs(v4u (&pv)[N], __amdgpu_buffer_rsrc_t rs, int base, bool hiHalf = false) {
  for (unsigned spin = 0; spin < SPIN_CAP; ++spin) {
#pragma unroll
    for (int i = 0; i < N; ++i) pv[i] = __builtin_amdgcn_raw_buffer_load_b128(rs, (N == 14 && i == 13 && hiHalf) ? OOB : base + i * 1024, 0, AUX_SC1);
    cfence();
    unsigned m = max4(pv[0]);
#pragma unroll
    for (int i = 1; i < N; ++i) m = max(m, max4(pv[i]));
    if (__all(m != SENT)) break;
    __builtin_amdgcn_s_sleep(SLP);
  }
}
template <int N> DI f32x4 own_reduce(__amdgpu_buffer_rsrc_t rs, int base, int lane) {
  v4u pv[N];
  poll_recs<N>(pv, rs, base + lane * 16, lane >= 32);
  f32x4 s = __builtin_bit_cast(f32x4, pv[0]);
#pragma unroll
  for (int i = 1; i < N; ++i) s += __builtin_bit_cast(f32x4, pv[i]);
#pragma unroll
  for (int k = 0; k < 4; ++k) { float t = s[k]; t += dpp<ROR8>(t); t += dpp<ROR4>(t); s[k] = xsum32(xsum16(t)); }
  return s;
}
DI f32x4 gather8(__amdgpu_buffer_rsrc_t rs, int base, int lane) {
  v4u pv[8];
  poll_recs<8>(pv, rs, base + lane * 16);
  f32x4 s = __builtin_bit_cast(f32x4, pv[0]);
#pragma unroll
  for (int i = 1; i < 8; ++i) s += __builtin_bit_cast(f32x4, pv[i]);
  return s;
}
DI void publish4(__amdgpu_buffer_rsrc_t rs, int off, const f32x4& s) {
  const v4u o = {sanef(s[0]), sanef(s[1]), sanef(s[2]), sanef(s[3])};
  __builtin_amdgcn_raw_buffer_store_b128(o, rs, off, 0, AUX_SC1);
}
DI void split4(const f32x4& v, v2u& hi, v2u& lo) {
  hi.x = cvtpk(v[0], v[1]); hi.y = cvtpk(v[2], v[3]);
  lo.x = cvtpk(v[0] - blo(hi.x), v[1] - bhi(hi.x)); lo.y = cvtpk(v[2] - blo(hi.y), v[3] - bhi(hi.y));
}
DI bf16x8 wfrag(LAS unsigned char* p, int pitch) {
  const s16x4 a = __builtin_amdgcn_ds_read_tr16_b64_v4i16((LAS s16x4*)p);
  const s16x4 b = __builtin_amdgcn_ds_read_tr16_b64_v4i16((LAS s16x4*)(p + 4 * pitch));
  return __builtin_shufflevector(a, b, 0, 1, 2, 3, 4, 5, 6, 7);
}
DI f32x4 mfma16(const bf16x8& a, const v4u& b, const f32x4& c) {
  return __builtin_amdgcn_mfma_f32_16x16x32_bf16(a, __builtin_bit_cast(bf16x8, b), c, 0, 0, 0);
}
DI f32x4 hilo(const f32x4& d) { f32x4 r; _Pragma("unroll") for (int k = 0; k < 4; ++k) r[k] = d[k] + dpp<XOR2>(d[k]); return r; }

__global__ void __launch_bounds__(NT, 3)
fused(const u16* __restrict__ x, const u16* __restrict__ ckv, const u16* __restrict__ wq, const u16* __restrict__ wuk,
      const u16* __restrict__ wuv, const u16* __restrict__ wo, const u16* __restrict__ wgt, const u16* __restrict__ wup,
      const u16* __restrict__ wdn, u16* __restrict__ out, unsigned char* __restrict__ ws) {
  extern __shared__ __attribute__((aligned(16))) unsigned char smem[];
  LAS unsigned char* L = (LAS unsigned char*)smem;
  LAS unsigned* prog = (LAS unsigned*)(L + L_CTRL);
  const int tid = threadIdx.x, lane = tid & 63, wv = __builtin_amdgcn_readfirstlane(tid >> 6);
  const int wg = blockIdx.x, xcd = wg & 7, jj = wg >> 3;
  const int h = xcd * 4 + (jj >> 3), cs = jj & 7;
  const int ab = wg >> 7, asplit = wg & 127;
  const bool ffn = jj < 27;
  const int cb = xcd * 9 + min(jj / 3, 8), kt = jj % 3, fi = cb * 3 + kt;
  const int nd = kt < 2 ? 24 : 16;
  const bool owner = jj >= 27 && (jj - 27) * 8 + xcd < 36;
  const int own = ((jj - 27) * 8 + xcd) * 2 + (wv & 1);
  const int rg = lane >> 4, ch = lane & 15;
  const __amdgpu_buffer_rsrc_t rs = __builtin_amdgcn_make_buffer_rsrc((void*)ws, 0, WS_BYTES, 0x00020000);
  const bool isC = wv < 4, isAdm = wv == 4, isDma = wv >= 8;
  if (tid < 32) prog[tid] = 0u;
  bar();

  u64 ep = 0;
  float xmean0 = 0.f, xmean1 = 0.f;
  v4u xa, xb, xc, wqv[5], wukv[4]; unsigned xo0, xo1;
  if (isAdm) {
    gload2sc(ep, ws, WS_EPOCH);
  } else if (isC) {
    const int m0 = 72 * cs + 18 * wv;
    gload2sc(ep, ws, WS_EPOCH);
    gload4(xa, x, lane * 16);
    gload4(xb, x, 1024 + lane * 16);
    gload4(xc, x, 2048 + (lane & 15) * 16);
    gloadh(xo0, x, (m0 + min(lane, 17)) * 2);
    gloadh(xo1, x, 1152 + (m0 + min(lane, 17)) * 2);
    const unsigned char* b = (const unsigned char*)wq + (size_t)m0 * 8192 + h * 256;
#pragma unroll
    for (int i = 0; i < 4; ++i) gload4nt(wqv[i], b + i * 4 * 8192, rg * 8192 + ch * 16);
    gload4nt(wqv[4], b, (16 + (rg & 1)) * 8192 + ch * 16);
    const unsigned char* b2 = (const unsigned char*)wuk + (size_t)(64 * cs + 16 * wv) * 8192 + h * 256;
#pragma unroll
    for (int i = 0; i < 4; ++i) gload4nt(wukv[i], b2 + i * 8192, (4 * rg) * 8192 + ch * 16);
  }
  if (isC) {
    waitv<9>();
    TIE(xa); TIE(xb); TIE(xc); TIE(xo0); TIE(xo1);
    float sa = 0.f, sb = 0.f, sc = 0.f;
#pragma unroll
    for (int k = 0; k < 4; ++k) { sa += blo(xa[k]) + bhi(xa[k]); sb += blo(xb[k]) + bhi(xb[k]); sc += blo(xc[k]) + bhi(xc[k]); }
    const bool b0 = lane < 8, c1 = lane < 16;
    const float m0 = wave_sum(sa + (b0 ? sb : 0.f)) * (1.0f / DM);
    const float m1 = wave_sum((b0 ? 0.f : sb) + (c1 ? sc : 0.f)) * (1.0f / DM);
    const float mb = b0 ? m0 : m1;
    xmean0 = m0; xmean1 = m1;
    float va = 0.f, vb = 0.f, vc = 0.f;
#pragma unroll
    for (int k = 0; k < 4; ++k) {
      float d;
      d = blo(xa[k]) - m0; va += d * d; d = bhi(xa[k]) - m0; va += d * d;
      d = blo(xb[k]) - mb; vb += d * d; d = bhi(xb[k]) - mb; vb += d * d;
      d = blo(xc[k]) - m1; vc += d * d; d = bhi(xc[k]) - m1; vc += d * d;
    }
    const float v0 = wave_sum(va + (b0 ? vb : 0.f)) * (1.0f / DM);
    const float v1 = wave_sum((b0 ? 0.f : vb) + (c1 ? vc : 0.f)) * (1.0f / DM);
    const float r0 = __builtin_amdgcn_rsqf(v0 + 1e-5f), r1 = __builtin_amdgcn_rsqf(v1 + 1e-5f);
    if (lane < 20) *(LAS f32x2*)(L + L_XL + (wv * 20 + lane) * 8) = lane < 18 ? f32x2{(blo(xo0) - m0) * r0, (blo(xo1) - m1) * r1} : f32x2{0.f, 0.f};
    waitl();
    f32x2 xr[5];
#pragma unroll
    for (int i = 0; i < 5; ++i) xr[i] = *(LAS f32x2*)(L + L_XL + (wv * 20 + 4 * i + rg) * 8);
    float a0[8], a1[8];
#pragma unroll
    for (int k = 0; k < 8; ++k) { a0[k] = 0.f; a1[k] = 0.f; }
    waitv<4>();
#pragma unroll
    for (int i = 0; i < 5; ++i) { TIE(wqv[i]); fma8(a0, a1, wqv[i], xr[i][0], xr[i][1]); }
    float t[8];
#pragma unroll
    for (int k = 0; k < 8; ++k) t[k] = tr32(a0[k], a1[k]);
    f32x4 o;
#pragma unroll
    for (int k = 0; k < 4; ++k) o[k] = tr16(t[k], t[k + 4]);
    *(LAS f32x4*)(L + L_QP + (wv * 256 + (lane >> 5) * 128 + ch * 8 + (rg & 1) * 4) * 4) = o;
    sig(prog, wv, 1u, lane);
  }

  if (!isC) {
    if (isAdm) {
      const v4u sv = {SENT, SENT, SENT, SENT};
#pragma unroll
      for (int i = 0; i < 9; ++i) gstore4sc(sv, ws, WS_CLR + wg * CLR_PER_WG + min(i * 1024 + lane * 16, CLR_PER_WG - 16));
      waitv<0>();
      TIE(ep);
      ep += 0x9E3779B97F4A7C15ull;
      const unsigned elo = (unsigned)ep, ehi = (unsigned)(ep >> 32);
      const v4u ev = {elo, ehi, elo, ehi};
      if (lane < NREP) gstore4sc(ev, ws, WS_FLAG + lane * 4096 + wg * 16);
      for (unsigned spin = 0; spin < SPIN_CAP; ++spin) {
        __builtin_amdgcn_s_sleep(16);
        v4u a[4];
#pragma unroll
        for (int i = 0; i < 4; ++i) a[i] = __builtin_amdgcn_raw_buffer_load_b128(rs, WS_FLAG + (wg & (NREP - 1)) * 4096 + i * 1024 + lane * 16, 0, AUX_SC1);
        cfence();
        unsigned d = 0;
#pragma unroll
        for (int i = 0; i < 4; ++i) d |= (a[i].x ^ elo) | (a[i].y ^ ehi) | (a[i].z ^ elo) | (a[i].w ^ ehi);
        TIE(d);
        if (__all(d == 0u)) break;
      }
      sig(prog, 12, 1u, lane);
    }
    if (isDma) {
      __builtin_amdgcn_s_sleep(8);
      const unsigned char* b = (const unsigned char*)ckv + ((size_t)ab * SK + asplit * 128) * 1024;
      const unsigned lb = (unsigned)(uintptr_t)(L + L_C);
      bool deep = false;
#pragma unroll
      for (int i = 0; i < 32; ++i) {
        const int r = 4 * i + (wv - 8);
        const int sw = ((r & 3) << 2) | ((r >> 2) & 3);
        if (i >= DMA_D0) {
          if (!deep) deep = __builtin_amdgcn_readfirstlane(*(volatile LAS unsigned*)(prog + 14)) >= 1u;
          if (!deep) waitv<DMA_D0 - 1>(); else if (i >= DMA_DEPTH) waitv<DMA_DEPTH - 1>();
        }
        dma16(b + r * 1024, (unsigned)(((lane & 48) | ((lane & 15) ^ sw)) * 16), lb + r * 1024);
      }
      sig(prog, wv, 1u, lane);
      waitv<0>();
      sig(prog, wv, 2u, lane);
    }
    for (;;) {
      v4u a = *(volatile LAS v4u*)(prog);
      if (__builtin_amdgcn_readfirstlane(min(min(a.x, a.y), min(a.z, a.w))) >= 4u) break;
      __builtin_amdgcn_s_sleep(2);
    }
    cfence();
    v4u wuvr[2], wor[3], wdr[4], wgur[6];
    const int td = tid - 256, wd = wv - 4;
    const int orp = min(lane / 9, 6), oc9 = lane % 9;
    const int gr = (wd >> 1) * 48 + (lane >> 3);
    const int dg = min(td / 72, 6), oc = td % 72;
    {
      const unsigned char* b = (const unsigned char*)wuv + (size_t)(64 * cs + 8 * wd) * 8192 + h * 256;
      gload4nt(wuvr[0], b, rg * 8192 + ch * 16);
      gload4nt(wuvr[1], b + 4 * 8192, rg * 8192 + ch * 16);
      const unsigned char* wob = (const unsigned char*)wo + (size_t)(h * 128 + 16 * wd) * 1152 + cs * 144;
      gload4nt(wor[0], wob, orp * 1152 + oc9 * 16);
      gload4nt(wor[1], wob, (7 + orp) * 1152 + oc9 * 16);
      gload4nt(wor[2], wob, min(14 + orp, 15) * 1152 + oc9 * 16);
    }
    const unsigned char* gup = (const unsigned char*)((wd & 1) ? wup : wgt) + (size_t)(192 * kt) * 9216 + cb * 128;
    if (ffn) {
#pragma unroll
      for (int i = 0; i < NB1; ++i) gload4nt(wgur[i], gup + (size_t)i * 8 * 9216, gr * 9216 + (lane & 7) * 16);
      waitv<NB1>();
    } else {
      waitv<0>();
    }
    for (;;) {
      v4u a = *(volatile LAS v4u*)(prog);
      if (__builtin_amdgcn_readfirstlane(min(min(a.x, a.y), min(a.z, a.w))) >= 7u) break;
      __builtin_amdgcn_s_sleep(2);
    }
    cfence();
    TIE(wuvr[0]); TIE(wuvr[1]); TIE(wor[0]); TIE(wor[1]); TIE(wor[2]);
    *(LAS v4u*)(L + I_UV + (8 * wd + rg) * 256 + ch * 16) = wuvr[0];
    *(LAS v4u*)(L + I_UV + (8 * wd + 4 + rg) * 256 + ch * 16) = wuvr[1];
    if (lane < 63) {
      *(LAS v4u*)(L + I_WO + (16 * wd + orp) * 144 + oc9 * 16) = wor[0];
      *(LAS v4u*)(L + I_WO + (16 * wd + 7 + orp) * 144 + oc9 * 16) = wor[1];
      if (orp < 2) *(LAS v4u*)(L + I_WO + (16 * wd + 14 + orp) * 144 + oc9 * 16) = wor[2];
    }
    sig(prog, wv, 3u, lane);
    if (ffn) {
      for (;;) {
        unsigned a = *(volatile LAS unsigned*)(prog + 13);
        if (__builtin_amdgcn_readfirstlane(a) >= 1u) break;
        __builtin_amdgcn_s_sleep(2);
      }
      cfence();
#pragma unroll
      for (int i = NB1; i < NB2; ++i) gload4nt(wgur[i], gup + (size_t)i * 8 * 9216, gr * 9216 + (lane & 7) * 16);
      for (;;) {
        unsigned a = *(volatile LAS unsigned*)(prog + 13);
        if (__builtin_amdgcn_readfirstlane(a) >= 2u) break;
        __builtin_amdgcn_s_sleep(2);
      }
      cfence();
#pragma unroll
      for (int i = NB2; i < 6; ++i) gload4nt(wgur[i], gup + (size_t)i * 8 * 9216, gr * 9216 + (lane & 7) * 16);
      const unsigned char* b3 = (const unsigned char*)wdn + (size_t)(cb * 64 + kt * 24) * 1152;
#pragma unroll
      for (int i = 0; i < 4; ++i) gload4nt(wdr[i], b3, min(dg + 7 * i, nd - 1) * 1152 + oc * 16);
      waitv<4>();
#pragma unroll
      for (int i = 0; i < 6; ++i) { TIE(wgur[i]); *(LAS v4u*)(L + I_GU + (gr + 8 * i) * 272 + (wd & 1) * 128 + (lane & 7) * 16) = wgur[i]; }
      sig(prog, wv, 4u, lane);
      waitv<0>();
      if (td < 504) {
#pragma unroll
        for (int i = 0; i < 4; ++i) { TIE(wdr[i]); *(LAS v4u*)(L + I_DN + (dg + 7 * i) * 1168 + oc * 16) = dg + 7 * i < nd ? wdr[i] : v4u{0u, 0u, 0u, 0u}; }
        if (dg < 4) *(LAS v4u*)(L + I_DN + (28 + dg) * 1168 + oc * 16) = v4u{0u, 0u, 0u, 0u};
      }
      sig(prog, wv, 5u, lane);
    }
    return;
  }

  TIE(ep);
  const unsigned elo = __builtin_amdgcn_readfirstlane((unsigned)ep), ehi = __builtin_amdgcn_readfirstlane((unsigned)(ep >> 32));
  const unsigned tag = elo ^ 0x5bd1e995u, saltA = ehi + elo * 0x85EBCA6Bu, saltB = (elo ^ ehi) * 0xC2B2AE35u + 0x27D4EB2Fu;
  if (wv == 0) {
    wait4(prog, 0, 1u);
    f32x4 s = *(LAS f32x4*)(L + L_QP + lane * 16);
#pragma unroll
    for (int w = 1; w < 4; ++w) s += *(LAS f32x4*)(L + L_QP + w * 1024 + lane * 16);
    {
      const v4u su = __builtin_bit_cast(v4u, s);
      __builtin_amdgcn_raw_buffer_store_b128(su, rs, WS_Q0 + (h * 8 + cs) * 1024 + lane * 16, 0, AUX_SC1);
      const v4u tr = {tag, wave_xor(xor4(su)) ^ saltA, wave_add(add4(su)) + saltB, tag};
      __builtin_amdgcn_raw_buffer_store_b128(tr, rs, lane == 0 ? WS_T0 + (h * 8 + cs) * 128 : OOB, 0, AUX_SC1);
    }
    v4u pv[8];
    for (unsigned spin = 0; spin < SPIN_CAP; ++spin) {
      const v4u tr = __builtin_amdgcn_raw_buffer_load_b128(rs, WS_T0 + (h * 8 + (lane & 7)) * 128, 0, AUX_SC1);
#pragma unroll
      for (int i = 0; i < 8; ++i) pv[i] = __builtin_amdgcn_raw_buffer_load_b128(rs, WS_Q0 + (h * 8 + i) * 1024 + lane * 16, 0, AUX_SC1);
      cfence();
      if (__all(tr.x == tag && tr.w == tag)) {
        unsigned vx[8], va[8];
#pragma unroll
        for (int i = 0; i < 8; ++i) { vx[i] = xor4(pv[i]); va[i] = add4(pv[i]); }
        unsigned cx = tred8<false>(vx, lane), ca = tred8<true>(va, lane);
        cx ^= dppu<ROR8>(cx); ca += dppu<ROR8>(ca);
        { auto t = __builtin_amdgcn_permlane16_swap(cx, cx, false, false); cx = t[0] ^ t[1]; }
        { auto t = __builtin_amdgcn_permlane16_swap(ca, ca, false, false); ca = t[0] + t[1]; }
        { auto t = __builtin_amdgcn_permlane32_swap(cx, cx, false, false); cx = t[0] ^ t[1]; }
        { auto t = __builtin_amdgcn_permlane32_swap(ca, ca, false, false); ca = t[0] + t[1]; }
        if (__all((cx ^ saltA) == tr.y && ca + saltB == tr.z)) break;
      }
      __builtin_amdgcn_s_sleep(SL0);
    }
    f32x4 qs = __builtin_bit_cast(f32x4, pv[0]);
#pragma unroll
    for (int i = 1; i < 8; ++i) qs += __builtin_bit_cast(f32x4, pv[i]);
    *(LAS f32x4*)(L + L_QV + lane * 16) = qs;
    sig(prog, 14, 1u, lane);
  } else {
    wait1(prog, 14, 1u);
  }
  {
    LAS unsigned char* qv = L + L_QV;
    const f32x4 q00 = *(LAS f32x4*)(qv + (ch * 8) * 4), q01 = *(LAS f32x4*)(qv + (ch * 8 + 4) * 4);
    const f32x4 q10 = *(LAS f32x4*)(qv + (128 + ch * 8) * 4), q11 = *(LAS f32x4*)(qv + (128 + ch * 8 + 4) * 4);
    float d0[4], d1[4];
    waitv<0>();
#pragma unroll
    for (int i = 0; i < 4; ++i) {
      TIE(wukv[i]);
      const v4u w = wukv[i];
      float s0 = blo(w[0]) * q00[0], s1 = blo(w[0]) * q10[0];
      s0 = fmaf(bhi(w[0]), q00[1], s0); s1 = fmaf(bhi(w[0]), q10[1], s1);
      s0 = fmaf(blo(w[1]), q00[2], s0); s1 = fmaf(blo(w[1]), q10[2], s1);
      s0 = fmaf(bhi(w[1]), q00[3], s0); s1 = fmaf(bhi(w[1]), q10[3], s1);
      s0 = fmaf(blo(w[2]), q01[0], s0); s1 = fmaf(blo(w[2]), q11[0], s1);
      s0 = fmaf(bhi(w[2]), q01[1], s0); s1 = fmaf(bhi(w[2]), q11[1], s1);
      s0 = fmaf(blo(w[3]), q01[2], s0); s1 = fmaf(blo(w[3]), q11[2], s1);
      s0 = fmaf(bhi(w[3]), q01[3], s0); s1 = fmaf(bhi(w[3]), q11[3], s1);
      d0[i] = sum_row16(s0); d1[i] = sum_row16(s1);
    }
    constexpr float QS = 0.08838834764831845f * 1.4426950408889634f;
    const bool sel = ch & 1;
    v2u o;
    o.x = cvtpk((sel ? d1[0] : d0[0]) * QS, (sel ? d1[1] : d0[1]) * QS);
    o.y = cvtpk((sel ? d1[2] : d0[2]) * QS, (sel ? d1[3] : d0[3]) * QS);
    if (ch < 2) *(LAS v2u*)(L + L_QO + (ch & 1) * 128 + (16 * wv + 4 * rg) * 2) = o;
  }
  sig(prog, wv, 3u, lane);
  if (wv == 0) {
    wait4(prog, 0, 3u);
    const v4u o = *(LAS v4u*)(L + L_QO + (lane & 15) * 16);
    __builtin_amdgcn_raw_buffer_store_b128(o, rs, lane < 16 ? WS_QA + (lane >> 3) * 32768 + h * 1024 + cs * 128 + (lane & 7) * 16 : OOB, 0, AUX_SC1);
    unsigned cx = xor4(o), ca = add4(o);
    cx ^= dppu<XOR1>(cx); cx ^= dppu<XOR2>(cx); cx ^= dppu<HMIR>(cx);
    ca += dppu<XOR1>(ca); ca += dppu<XOR2>(ca); ca += dppu<HMIR>(ca);
    const v4u tr = {tag, cx ^ saltA, ca + saltB, tag};
    __builtin_amdgcn_raw_buffer_store_b128(tr, rs, (lane & 7) < 2 ? WS_T1 + ((lane >> 4) + 4 * (lane & 1)) * 8192 + ((lane >> 3) & 1) * 4096 + (h * 8 + cs) * 16 : OOB, 0, AUX_SC1);
    sig(prog, 14, 2u, lane);
  } else {
    wait1(prog, 14, 2u);
  }
  wait4(prog, 8, 1u);
  {
    const int base = WS_QA + ab * 32768;
    v4u qf[8];
    for (unsigned spin = 0; spin < SPIN_CAP; ++spin) {
      const v4u tr = __builtin_amdgcn_raw_buffer_load_b128(rs, WS_T1 + (wg & (NREP - 1)) * 8192 + ab * 4096 + ((8 * wv + (lane & 7)) * 8 + (lane >> 3)) * 16, 0, AUX_SC1);
#pragma unroll
      for (int i = 0; i < 8; ++i) qf[i] = __builtin_amdgcn_raw_buffer_load_b128(rs, base + (8 * wv + i) * 1024 + lane * 16, 0, AUX_SC1);
      cfence();
      if (__all(tr.x == tag && tr.w == tag)) {
        unsigned vx[8], va[8];
#pragma unroll
        for (int i = 0; i < 8; ++i) { vx[i] = xor4(qf[i]); va[i] = add4(qf[i]); }
        const unsigned cx = tred8<false>(vx, lane), ca = tred8<true>(va, lane);
        if (__all((cx ^ saltA) == tr.y && ca + saltB == tr.z)) break;
      }
      __builtin_amdgcn_s_sleep(SL1);
    }
    if (lane < 62) {
#pragma unroll
      for (int i = 0; i < 8; ++i) *(LAS v4u*)(L + L_S + (8 * wv + i) * QPITCH + ((lane ^ (wv >> 1)) * 16)) = qf[i];
    }
  }
  sig(prog, wv, 4u, lane);
  wait4(prog, 0, 4u);
  const v4u q31 = __builtin_amdgcn_raw_buffer_load_b128(rs, WS_QA + ab * 32768 + (lane & 31) * 1024 + 992 + (lane >> 5) * 16, 0, AUX_SC1);
  wait4(prog, 8, 2u);
  const int r31 = lane & 31, hh = lane >> 5;
  f32x16 sacc;
#pragma unroll
  for (int i = 0; i < 16; ++i) sacc[i] = 0.f;
  {
    const int row = 32 * wv + r31;
    const int sw = ((row & 3) << 2) | ((row >> 2) & 3);
    LAS unsigned char* arow = L + L_C + row * 1024;
    LAS unsigned char* brow = L + L_S + r31 * QPITCH + ((hh ^ (r31 >> 4)) * 16);
    constexpr int W = 6;
    v4u af[32], bf[32];
#pragma unroll
    for (int s = 0; s < W; ++s) {
      const int c64 = 2 * s + hh;
      af[s] = *(LAS v4u*)(arow + (c64 >> 4) * 256 + (((c64 & 15) ^ sw) * 16));
      bf[s] = *(LAS v4u*)(brow + s * 32);
    }
    __builtin_amdgcn_sched_barrier(0);
#pragma unroll
    for (int s = 0; s < 32; ++s) {
      if (s + W < 32) {
        const int c64 = 2 * (s + W) + hh;
        af[s + W] = *(LAS v4u*)(arow + (c64 >> 4) * 256 + (((c64 & 15) ^ sw) * 16));
        bf[s + W] = s + W < 31 ? *(LAS v4u*)(brow + (s + W) * 32) : q31;
      }
      sacc = __builtin_amdgcn_mfma_f32_32x32x16_bf16(__builtin_bit_cast(bf16x8, af[s]), __builtin_bit_cast(bf16x8, bf[s]), sacc, 0, 0, 0);
      __builtin_amdgcn_sched_barrier(0);
    }
  }
  float mx = sacc[0];
#pragma unroll
  for (int i = 1; i < 16; ++i) mx = fmaxf(mx, sacc[i]);
  mx = xmax32(mx);
  if (lane < 32) *(LAS float*)(L + L_MX + (wv * 32 + lane) * 4) = mx;
  sig(prog, wv, 5u, lane);
  wait4(prog, 0, 5u);
#pragma unroll
  for (int w = 0; w < 4; ++w) mx = fmaxf(mx, *(LAS float*)(L + L_MX + (w * 32 + r31) * 4));
  {
    float ls = 0.f;
    v4u pf[2];
#pragma unroll
    for (int i = 0; i < 8; ++i) {
      const float p0 = __builtin_amdgcn_exp2f(sacc[2 * i] - mx), p1 = __builtin_amdgcn_exp2f(sacc[2 * i + 1] - mx);
      const unsigned pk = cvtpk(p0, p1);
      ls += blo(pk) + bhi(pk);
      pf[i >> 2][i & 3] = pk;
    }
    ls = xsum32(ls);
    *(LAS v4u*)(L + L_P + (wv * 2 + 0) * 1024 + lane * 16) = pf[0];
    *(LAS v4u*)(L + L_P + (wv * 2 + 1) * 1024 + lane * 16) = pf[1];
    if (lane < 32) *(LAS float*)(L + L_LS + (wv * 32 + lane) * 4) = ls;
  }
  sig(prog, wv, 6u, lane);
  wait4(prog, 0, 6u);
  float lt = 0.f;
  {
    f32x16 oacc[4];
#pragma unroll
    for (int cb = 0; cb < 4; ++cb)
#pragma unroll
      for (int i = 0; i < 16; ++i) oacc[cb][i] = 0.f;
    const int blk = (lane >> 4) & 1, q4 = (lane & 15) >> 2, p4 = lane & 3;
    LAS unsigned char* tb = L + L_C + (4 * hh + q4) * 1024 + wv * 256 + 8 * (p4 & 1);
    v4u pb[8];
#pragma unroll
    for (int ks = 0; ks < 8; ++ks) pb[ks] = *(LAS v4u*)(L + L_P + ks * 1024 + lane * 16);
#pragma unroll
    for (int w = 0; w < 4; ++w) lt += *(LAS float*)(L + L_LS + (w * 32 + r31) * 4);
    const float inv = __builtin_amdgcn_rcpf(lt);
    LAS unsigned char* st = L + L_ST + wv * 4096;
    LAS unsigned char* stw = st + r31 * 128 + hh * 8;
    LAS unsigned char* str_ = st + (lane >> 3) * 128 + (((lane & 7) ^ (lane >> 3)) * 16);
    const int pofs = WS_PART + ((((lane >> 3) * 8 + 2 * wv) * 2 + ab) * 128 + asplit) * 128 + (lane & 7) * 16;
    constexpr int W = 5;
    s16x4 a[32][2];
    v4u ov[4];
#define CBK(n) (2 * ((n) >> 4) + ((n) & 1))
#define KSK(n) (((n) >> 1) & 7)
#define TRLD(n) { _Pragma("unroll") for (int t = 0; t < 2; ++t) { \
      const int chunk = ((CBK(n) ^ q4) << 2) | ((2 * blk + (p4 >> 1)) ^ (2 * t + hh)); \
      a[n][t] = __builtin_amdgcn_ds_read_tr16_b64_v4i16((LAS s16x4*)(tb + (16 * KSK(n) + 8 * t) * 1024 + chunk * 16)); } }
#define STG(half, k) { const f32x16& o = oacc[2 * (half) + ((k) >> 2)]; const int g = (k) & 3; v2u pk; \
      pk.x = cvtpk(o[4 * g] * inv, o[4 * g + 1] * inv); pk.y = cvtpk(o[4 * g + 2] * inv, o[4 * g + 3] * inv); \
      *(LAS v2u*)(stw + (((k) ^ (r31 & 7)) * 16)) = pk; }
#pragma unroll
    for (int n = 0; n < W; ++n) TRLD(n)
    __builtin_amdgcn_sched_barrier(0);
#pragma unroll
    for (int n = 0; n < 32; ++n) {
      if (n + W < 32) TRLD(n + W)
      const bf16x8 A = __builtin_shufflevector(a[n][0], a[n][1], 0, 1, 2, 3, 4, 5, 6, 7);
      oacc[CBK(n)] = __builtin_amdgcn_mfma_f32_32x32x16_bf16(A, __builtin_bit_cast(bf16x8, pb[KSK(n)]), oacc[CBK(n)], 0, 0, 0);
      if (n >= 18 && n < 26) STG(0, n - 18)
      if (n == 27) {
        sig(prog, wv, 7u, lane);
#pragma unroll
        for (int i = 0; i < 4; ++i) ov[i] = *(LAS v4u*)(str_ + i * 1024);
      }
      if (n == 30) {
#pragma unroll
        for (int i = 0; i < 4; ++i) __builtin_amdgcn_raw_buffer_store_b128(ov[i], rs, pofs + i * (8 * 8 * 2 * 128 * 128), 0, AUX_SC1);
      }
      __builtin_amdgcn_sched_barrier(0);
    }
#pragma unroll
    for (int k = 0; k < 8; ++k) STG(1, k)
    waitl();
#pragma unroll
    for (int i = 0; i < 4; ++i) ov[i] = *(LAS v4u*)(str_ + i * 1024);
#pragma unroll
    for (int i = 0; i < 4; ++i) __builtin_amdgcn_raw_buffer_store_b128(ov[i], rs, pofs + i * (8 * 8 * 2 * 128 * 128) + 2 * 128 * 128, 0, AUX_SC1);
    cfence();
#undef TRLD
#undef STG
#undef CBK
#undef KSK
    sig(prog, wv, 8u, lane);
    const unsigned lu = sanef(mx + __builtin_amdgcn_logf(lt));
    wait1(prog, 12, 1u);
    waitv<0>();
    if (lane < 32) __builtin_amdgcn_raw_buffer_store_b128(v4u{lu, lu, lu, lu}, rs, WS_LSE + (((lane * 4 + wv) * 2 + ab) * 128 + asplit) * 16, 0, AUX_SC1);
  }
  const int tq = ch >> 2, tp = lane & 3;
  const int trow = 8 * rg + tq, tcol = (tp >> 1) * 16 + (tp & 1) * 8;
  const f32x4 z4 = {0.f, 0.f, 0.f, 0.f};
  wait4(prog, 4, 3u); wait4(prog, 8, 3u);
  bf16x8 auv[2][2], awo[4], awo4[4];
#pragma unroll
  for (int ks = 0; ks < 2; ++ks)
#pragma unroll
    for (int t = 0; t < 2; ++t) auv[ks][t] = wfrag(L + I_UV + (32 * ks + trow) * 256 + (2 * wv + t) * 32 + tcol, 256);
#pragma unroll
  for (int ks = 0; ks < 4; ++ks) {
    awo[ks] = wfrag(L + I_WO + (32 * ks + trow) * 144 + wv * 32 + tcol, 144);
    awo4[ks] = wfrag(L + I_WO + (32 * ks + trow) * 144 + 128 + tcol, 144);
  }
  {
    const int rbase = WS_PART + ((h * 8 + cs) * 2 + (wv >> 1)) * 128 * 128;
    const int lbase = WS_LSE + ((h * 4 + (cs >> 1)) * 2 + (wv >> 1)) * 2048 + lane * 16;
    v4u pv[8];
    const int pbase = rbase + 64 * (wv & 1) * 128 + lane * 16;
    if ((wv & 1) == 0) {
      unsigned l0, l1;
      for (unsigned spin = 0; spin < SPIN_CAP; ++spin) {
        l0 = __builtin_amdgcn_raw_buffer_load_b32(rs, lbase, 0, AUX_SC1);
        l1 = __builtin_amdgcn_raw_buffer_load_b32(rs, lbase + 1024, 0, AUX_SC1);
        cfence();
        if (__all(max(l0, l1) != SENT)) break;
        __builtin_amdgcn_s_sleep(SL2);
      }
      sig(prog, 18 + (wv >> 1), 1u, lane);
#pragma unroll
      for (int i = 0; i < 8; ++i) pv[i] = __builtin_amdgcn_raw_buffer_load_b128(rs, pbase + i * 1024, 0, AUX_SC1);
      const float m = xmax32(xmax16(max_row16(fmaxf(asf(l0), asf(l1)))));
      const float e0 = __builtin_amdgcn_exp2f(asf(l0) - m), e1 = __builtin_amdgcn_exp2f(asf(l1) - m);
      const float it = __builtin_amdgcn_rcpf(wave_sum(e0 + e1));
      wait4(prog, 0, 8u);
      *(LAS float*)(L + T_WG + wv * 256 + lane * 4) = e0 * it;
      *(LAS float*)(L + T_WG + wv * 256 + 256 + lane * 4) = e1 * it;
      sig(prog, 18 + (wv >> 1), 2u, lane);
    } else {
      wait1(prog, 18 + (wv >> 1), 1u);
#pragma unroll
      for (int i = 0; i < 8; ++i) pv[i] = __builtin_amdgcn_raw_buffer_load_b128(rs, pbase + i * 1024, 0, AUX_SC1);
      wait1(prog, 18 + (wv >> 1), 2u);
    }
    float acc[8];
#pragma unroll
    for (int k = 0; k < 8; ++k) acc[k] = 0.f;
#pragma unroll
    for (int i = 0; i < 8; ++i) {
      const float w = *(LAS float*)(L + T_WG + wv * 256 + (8 * i + (lane >> 3)) * 4);
#pragma unroll
      for (int k = 0; k < 4; ++k) { acc[2 * k] = fmaf(w, blo(pv[i][k]), acc[2 * k]); acc[2 * k + 1] = fmaf(w, bhi(pv[i][k]), acc[2 * k + 1]); }
    }
    float t[4], u2[2];
#pragma unroll
    for (int k = 0; k < 4; ++k) t[k] = tr32(acc[k], acc[k + 4]);
#pragma unroll
    for (int k = 0; k < 2; ++k) u2[k] = tr16(t[k], t[k + 2]);
    u2[0] += dpp<ROR8>(u2[0]); u2[1] += dpp<ROR8>(u2[1]);
    if ((lane & 8) == 0) *(LAS f32x2*)(L + T_CC + (wv * 64 + (lane & 7) * 8 + (lane >> 5) * 4 + ((lane >> 4) & 1) * 2) * 4) = f32x2{u2[0], u2[1]};
  }
  sig(prog, wv, 9u, lane);
  wait4(prog, 0, 9u);
  {
    f32x4 c[2][4];
#pragma unroll
    for (int ks = 0; ks < 2; ++ks) {
      LAS unsigned char* cp = L + T_CC + ((ch & 1) * 128 + 32 * ks + 8 * rg) * 4;
      c[ks][0] = *(LAS f32x4*)(cp); c[ks][1] = *(LAS f32x4*)(cp + 256); c[ks][2] = *(LAS f32x4*)(cp + 16); c[ks][3] = *(LAS f32x4*)(cp + 272);
    }
    f32x4 d[2] = {z4, z4};
#pragma unroll
    for (int ks = 0; ks < 2; ++ks) {
      v2u h0, l0, h1, l1;
      split4(c[ks][0] + c[ks][1], h0, l0); split4(c[ks][2] + c[ks][3], h1, l1);
      const v4u b = (ch & 2) ? v4u{l0.x, l0.y, l1.x, l1.y} : v4u{h0.x, h0.y, h1.x, h1.y};
#pragma unroll
      for (int t = 0; t < 2; ++t) d[t] = mfma16(auv[ks][t], b, d[t]);
    }
#pragma unroll
    for (int t = 0; t < 2; ++t) publish4(rs, ch < 2 ? WS_CX + (h * 8 + cs) * 1024 + (ch * 128 + 32 * wv + 16 * t + 4 * rg) * 4 : OOB, hilo(d[t]));
  }
  if (wv == 0) sig(prog, 13, 1u, lane);
  if (wv == 0) {
    __builtin_amdgcn_s_sleep(PRE_G);
    const f32x4 cx = gather8(rs, WS_CX + h * 8192, lane);
    v2u hi, lo;
    split4(cx, hi, lo);
    *(LAS v2u*)(L + T_CX + hh * 272 + r31 * 8) = hi;
    *(LAS v2u*)(L + T_CX + (2 + hh) * 272 + r31 * 8) = lo;
    sig(prog, 14, 3u, lane);
  } else {
    wait1(prog, 14, 3u);
  }
  {
    v4u b[4];
#pragma unroll
    for (int ks = 0; ks < 4; ++ks) b[ks] = *(LAS v4u*)(L + T_CX + (ch & 3) * 272 + (32 * ks + 8 * rg) * 2);
    f32x4 d = z4, d4 = z4;
#pragma unroll
    for (int ks = 0; ks < 4; ++ks) { d = mfma16(awo[ks], b[ks], d); d4 = mfma16(awo4[ks], b[ks], d4); }
    const int ob = WS_OP + (ch * 32 + h) * 2304 + (72 * cs + 4 * rg) * 4;
    publish4(rs, ch < 2 ? ob + 64 * wv : OOB, hilo(d));
    publish4(rs, (ch < 2 && rg < 2 && wv == 2) ? ob + 256 : OOB, hilo(d4));
  }
  if (wv == 0) sig(prog, 13, 2u, lane);
  if (owner) {
    if (wv < 2) {
      const v2u xv = *(const v2u*)(x + own * 16 + (lane & 3) * 4);
      __builtin_amdgcn_s_sleep(PRE_H4);
      f32x4 s;
      {
        v4u pv[2];
        const int ob = WS_OP + ((own >= 36 ? 32 : 0) + (lane >> 2)) * 2304 + (own % 36) * 64 + (lane & 3) * 16;
        for (unsigned spin = 0; spin < SPIN_CAP; ++spin) {
          pv[0] = __builtin_amdgcn_raw_buffer_load_b128(rs, ob, 0, AUX_SC1);
          pv[1] = __builtin_amdgcn_raw_buffer_load_b128(rs, ob + 16 * 2304, 0, AUX_SC1);
          cfence();
          if (__all(max(max4(pv[0]), max4(pv[1])) != SENT)) break;
          __builtin_amdgcn_s_sleep(SLP);
        }
        s = __builtin_bit_cast(f32x4, pv[0]) + __builtin_bit_cast(f32x4, pv[1]);
#pragma unroll
        for (int k = 0; k < 4; ++k) { float t = s[k]; t += dpp<ROR8>(t); t += dpp<ROR4>(t); s[k] = xsum32(xsum16(t)); }
      }
      const f32x4 hown = f32x4{blo(xv.x) + s[0], bhi(xv.x) + s[1], blo(xv.y) + s[2], bhi(xv.y) + s[3]};
      const f32x4 s2 = own_reduce<14>(rs, WS_DP + own * (NFF * 64), lane);
      v2u o;
      o.x = cvtpk(hown[0] + s2[0], hown[1] + s2[1]);
      o.y = cvtpk(hown[2] + s2[2], hown[3] + s2[3]);
      if (lane < 4) *(v2u*)(out + own * 16 + lane * 4) = o;
      if (own == 0 && lane == 0)
        __hip_atomic_store((u64*)(ws + WS_EPOCH), ep + 1ull, __ATOMIC_RELAXED, __HIP_MEMORY_SCOPE_AGENT);
    }
  } else if (ffn) {
    const int fb = wv >> 1;
    const v2u xv = *(const v2u*)(x + fb * 576 + 192 * kt + min(lane, 47) * 4);
    if (wv == 3) {
      if (lane < 24) *(LAS v4u*)(L + T_XN + 4 * 400 + lane * 16) = v4u{0x3F803F80u, 0x3F803F80u, 0x3F803F80u, 0x3F803F80u};
#pragma unroll
      for (int r = 5; r < 8; ++r) if (lane < 24) *(LAS v4u*)(L + T_XN + r * 400 + lane * 16) = v4u{0u, 0u, 0u, 0u};
    }
    f32x4 hs;
    {
      v4u pv[16];
      const int ob = lane < 48 ? WS_OP + (fb * 32 + 16 * (wv & 1)) * 2304 + 768 * kt + lane * 16 : OOB;
      __builtin_amdgcn_s_sleep(PRE_G);
      for (unsigned spin = 0; spin < SPIN_CAP; ++spin) {
#pragma unroll
        for (int i = 0; i < 16; ++i) pv[i] = __builtin_amdgcn_raw_buffer_load_b128(rs, ob + i * 2304, 0, AUX_SC1);
        cfence();
        unsigned m = max4(pv[0]);
#pragma unroll
        for (int i = 1; i < 16; ++i) m = max(m, max4(pv[i]));
        if (__all(m != SENT)) break;
        __builtin_amdgcn_s_sleep(SLH);
      }
      hs = __builtin_bit_cast(f32x4, pv[0]);
#pragma unroll
      for (int i = 1; i < 16; ++i) hs += __builtin_bit_cast(f32x4, pv[i]);
    }
    if (wv & 1) {
      if (lane < 48) *(LAS f32x4*)(L + T_HP + fb * 768 + lane * 16) = hs;
      sig(prog, 16 + fb, 1u, lane);
    } else {
      const float cm = fb ? xmean1 : xmean0;
      hs += f32x4{blo(xv.x) - cm, bhi(xv.x) - cm, blo(xv.y) - cm, bhi(xv.y) - cm};
      wait1(prog, 16 + fb, 1u);
      hs += *(LAS f32x4*)(L + T_HP + fb * 768 + min(lane, 47) * 16);
      if (lane >= 48) hs = z4;
      v2u hi, lo;
      split4(hs, hi, lo);
      if (lane < 48) {
        *(LAS v2u*)(L + T_XN + fb * 400 + lane * 8) = hi;
        *(LAS v2u*)(L + T_XN + (2 + fb) * 400 + lane * 8) = lo;
      }
      sig(prog, 16 + fb, 2u, lane);
      const float s1 = wave_sum((hs[0] + hs[1]) + (hs[2] + hs[3]));
      const float s2 = wave_sum((hs[0] * hs[0] + hs[1] * hs[1]) + (hs[2] * hs[2] + hs[3] * hs[3]));
      __builtin_amdgcn_raw_buffer_store_b64(v2u{sanef(s1), sanef(s2)}, rs, lane == 0 ? WS_GX + fi * GXREC + 1536 + fb * 8 : OOB, 0, AUX_SC1);
    }
    wait4(prog, 4, 4u); wait4(prog, 8, 4u);
    bf16x8 wa[9];
    {
      bf16x8 ag[6], au[6];
      LAS unsigned char* ap = L + I_GU + trow * 272 + wv * 32 + tcol;
#pragma unroll
      for (int ks = 0; ks < 6; ++ks) { ag[ks] = wfrag(ap + ks * 32 * 272, 272); au[ks] = wfrag(ap + ks * 32 * 272 + 128, 272); }
      wait1(prog, 16, 2u); wait1(prog, 17, 2u);
      f32x4 dg_ = z4, du_ = z4;
      LAS unsigned char* bp = L + T_XN + (ch & 7) * 400 + 8 * rg * 2;
      v4u b[6];
#pragma unroll
      for (int ks = 0; ks < 6; ++ks) b[ks] = *(LAS v4u*)(bp + ks * 64);
#pragma unroll
      for (int ks = 0; ks < 6; ++ks) { dg_ = mfma16(ag[ks], b[ks], dg_); du_ = mfma16(au[ks], b[ks], du_); }
      const int rb = WS_GX + fi * GXREC + (16 * wv + 4 * rg) * 4;
      publish4(rs, ch < 2 ? rb + ch * 256 : (ch == 4 ? rb + 1024 : OOB), hilo(dg_));
      publish4(rs, ch < 2 ? rb + 512 + ch * 256 : (ch == 4 ? rb + 1280 : OOB), hilo(du_));
      wait4(prog, 4, 5u); wait4(prog, 8, 5u);
#pragma unroll
      for (int t = 0; t < 9; ++t) wa[t] = wfrag(L + I_DN + trow * 1168 + (9 * wv + t) * 32 + tcol, 1168);
    }
    if (wv == 0) {
      v4u pv[3], pc[3]; v2u ps[3];
      const int gb = WS_GX + cb * 3 * GXREC;
      __builtin_amdgcn_s_sleep(PRE_G);
      for (unsigned spin = 0; spin < SPIN_CAP; ++spin) {
#pragma unroll
        for (int i = 0; i < 3; ++i) {
          pv[i] = __builtin_amdgcn_raw_buffer_load_b128(rs, gb + i * GXREC + lane * 16, 0, AUX_SC1);
          pc[i] = __builtin_amdgcn_raw_buffer_load_b128(rs, gb + i * GXREC + 1024 + hh * 256 + ch * 16, 0, AUX_SC1);
          ps[i] = __builtin_amdgcn_raw_buffer_load_b64(rs, gb + i * GXREC + 1536 + ((lane >> 4) & 1) * 8, 0, AUX_SC1);
        }
        cfence();
        unsigned m = 0;
#pragma unroll
        for (int i = 0; i < 3; ++i) m = max(max(m, max4(pv[i])), max(max4(pc[i]), max(ps[i].x, ps[i].y)));
        if (__all(m != SENT)) break;
        __builtin_amdgcn_s_sleep(SLP);
      }
      const float mu = (asf(ps[0].x) + asf(ps[1].x) + asf(ps[2].x)) * (1.0f / DM);
      const float var = fmaxf((asf(ps[0].y) + asf(ps[1].y) + asf(ps[2].y)) * (1.0f / DM) - mu * mu, 0.f);
      const float r = __builtin_amdgcn_rsqf(var + 1e-5f);
      const f32x4 csum = __builtin_bit_cast(f32x4, pc[0]) + __builtin_bit_cast(f32x4, pc[1]) + __builtin_bit_cast(f32x4, pc[2]);
      const f32x4 sgu = (__builtin_bit_cast(f32x4, pv[0]) + __builtin_bit_cast(f32x4, pv[1]) + __builtin_bit_cast(f32x4, pv[2]) - csum * mu) * r;
      f32x4 a;
#pragma unroll
      for (int k = 0; k < 4; ++k) {
        auto t = __builtin_amdgcn_permlane32_swap(asu(sgu[k]), asu(sgu[k]), false, false);
        const float g = asf(t[0]), u = asf(t[1]);
        a[k] = g * __builtin_amdgcn_rcpf(1.0f + __expf(-g)) * u;
      }
      v2u hi, lo;
      split4(a, hi, lo);
      *(LAS v2u*)(L + T_ACT + (2 * hh + ((lane >> 4) & 1)) * 176 + ch * 8) = hh ? lo : hi;
      if (lane < 16) *(LAS v2u*)(L + T_ACT + (lane >> 2) * 176 + 128 + (lane & 3) * 8) = v2u{0u, 0u};
      sig(prog, 14, 5u, lane);
    } else {
      wait1(prog, 14, 5u);
    }
    {
      const v4u b = *(LAS v4u*)(L + T_ACT + (ch & 3) * 176 + (24 * kt + 8 * rg) * 2);
      const int ob = ch < 2 ? WS_DP + ((ch * 36 + 9 * wv) * NFF + fi) * 64 + rg * 16 : OOB;
      f32x4 d[9];
#pragma unroll
      for (int t = 0; t < 9; ++t) d[t] = mfma16(wa[t], b, z4);
#pragma unroll
      for (int t = 0; t < 9; ++t) publish4(rs, ob + t * (NFF * 64), hilo(d[t]));
    }
  }
}
}

extern "C" void kernel_launch(void* const* d_in, const int* in_sizes, int n_in,
                              void* d_out, int out_size, void* d_ws, size_t ws_size,
                              hipStream_t stream) {
  using mk::u16;
  (void)hipFuncSetAttribute((const void*)mk::fused, hipFuncAttributeMaxDynamicSharedMemorySize, mk::L_BYTES);
  mk::fused<<<mk::NWG, mk::NT, mk::L_BYTES, stream>>>(
      (const u16*)d_in[0], (const u16*)d_in[1], (const u16*)d_in[2], (const u16*)d_in[3], (const u16*)d_in[4], (const u16*)d_in[5],
      (const u16*)d_in[7], (const u16*)d_in[8], (const u16*)d_in[9], (u16*)d_out, (unsigned char*)d_ws);
}
```

```cpp
#include <hip/hip_runtime.h>
#include <hip/hip_bf16.h>
#include <stdint.h>

namespace mk {
constexpr int SK = 16384, DM = 576;
constexpr int NWG = 256, NT = 768;
constexpr int DMA_DEPTH = 16, DMA_D0 = 2;
constexpr int H1_TRIG = 26;
constexpr int NB1 = 2, NB2 = 5;
constexpr int SL0 = 4, SL1 = 4, SL2 = 4, SLP = 2, SLH = 4, PRE_0 = 16, PRE_G = 16, PRE_H4 = 64;
constexpr int NOWN = 72;
constexpr int NFF = 216;

constexpr int WS_EPOCH = 0;
constexpr int NREP = 8;
constexpr int WS_QA = 65536;
constexpr int WS_Q0 = WS_QA + 65536;
constexpr int WS_T0 = WS_Q0 + 262144;
constexpr int WS_T1 = WS_T0 + 32768;
constexpr int WS_CLR = WS_T1 + NREP * 8192;
constexpr int WS_CX = WS_CLR;
constexpr int WS_OP = WS_CX + 262144;
constexpr int WS_DP = WS_OP + 2 * 32 * 2304;
constexpr int GXREC = 1664;
constexpr int WS_GX = WS_DP + NOWN * NFF * 64;
constexpr int WS_CLR_END = WS_GX + NFF * GXREC + 1024;
constexpr int CLR_PER_WG = (WS_CLR_END - WS_CLR) / NWG;
static_assert(CLR_PER_WG * NWG == WS_CLR_END - WS_CLR && CLR_PER_WG % 16 == 0 && CLR_PER_WG <= 7 * 1024, "clear split");
constexpr int WS_LSE = 3 << 20;
static_assert(WS_CLR_END <= WS_LSE, "ws map");
constexpr int WS_PART = 4 << 20;
constexpr int WS_BYTES = 32 << 20;

constexpr int L_C = 0;
constexpr int L_S = 131072;
constexpr int L_CTRL = 162816;
constexpr int L_BYTES = 163840;
constexpr int L_XL = L_S;
constexpr int L_QP = L_S + 5120;
constexpr int L_QV = L_S + 13312;
constexpr int L_QO = L_S + 14336;
constexpr int QPITCH = 992;
constexpr int L_P = L_S;
constexpr int L_LS = L_S + 8192;
constexpr int L_MX = L_CTRL + 128;
constexpr int I_UV = 0;
constexpr int I_WO = 16384;
constexpr int I_GU = 34944;
constexpr int I_DN = 88064;
constexpr int L_ST = 145536;
constexpr int T_WG = L_ST;
constexpr int T_CC = L_ST + 1024;
constexpr int T_CX = L_ST + 2048;
constexpr int T_XN = L_ST + 4096;
constexpr int T_HP = L_ST + 7424;
constexpr int T_ACT = L_ST + 9216;
static_assert(I_GU + 192 * 272 <= I_DN && I_DN + 32 * 1168 <= L_S && L_ST + 16384 <= L_CTRL, "LDS map");

#define LAS __attribute__((address_space(3)))
typedef unsigned v4u __attribute__((ext_vector_type(4)));
typedef unsigned v2u __attribute__((ext_vector_type(2)));
typedef float f32x4 __attribute__((ext_vector_type(4)));
typedef float f32x2 __attribute__((ext_vector_type(2)));
typedef float f32x16 __attribute__((ext_vector_type(16)));
typedef short bf16x8 __attribute__((ext_vector_type(8)));
typedef short s16x4 __attribute__((ext_vector_type(4)));
typedef unsigned short u16;
typedef unsigned long long u64;
#define DI __device__ __forceinline__
constexpr int AUX_SC1 = 16;
constexpr unsigned SENT = 0xFFFFFFFFu;
constexpr unsigned SPIN_CAP = 1u << 16;

DI float asf(unsigned u) { return __builtin_bit_cast(float, u); }
DI unsigned asu(float f) { return __builtin_bit_cast(unsigned, f); }
DI float blo(unsigned u) { return asf(u << 16); }
DI float bhi(unsigned u) { return asf(u & 0xffff0000u); }
typedef __bf16 bf16x2v __attribute__((ext_vector_type(2)));
DI unsigned cvtpk(float lo, float hi) { return __builtin_bit_cast(unsigned, __builtin_convertvector(f32x2{lo, hi}, bf16x2v)); }
DI unsigned sanef(float f) { unsigned u = asu(f); return u == SENT ? 0x7FC00000u : u; }
template <int CTRL> DI float dpp(float x) { return asf((unsigned)__builtin_amdgcn_mov_dpp((int)asu(x), CTRL, 0xf, 0xf, true)); }
constexpr int XOR1 = 0xB1, XOR2 = 0x4E, HMIR = 0x141, MIR = 0x140, ROR4 = 0x124, ROR8 = 0x128;
DI float sum_row16(float x) { x += dpp<XOR1>(x); x += dpp<XOR2>(x); x += dpp<HMIR>(x); x += dpp<MIR>(x); return x; }
DI float max_row16(float x) { x = fmaxf(x, dpp<XOR1>(x)); x = fmaxf(x, dpp<XOR2>(x)); x = fmaxf(x, dpp<HMIR>(x)); x = fmaxf(x, dpp<MIR>(x)); return x; }
DI float xsum16(float x) { auto s = __builtin_amdgcn_permlane16_swap(asu(x), asu(x), false, false); return asf(s[0]) + asf(s[1]); }
DI float xsum32(float x) { auto s = __builtin_amdgcn_permlane32_swap(asu(x), asu(x), false, false); return asf(s[0]) + asf(s[1]); }
DI float xmax16(float x) { auto s = __builtin_amdgcn_permlane16_swap(asu(x), asu(x), false, false); return fmaxf(asf(s[0]), asf(s[1])); }
DI float xmax32(float x) { auto s = __builtin_amdgcn_permlane32_swap(asu(x), asu(x), false, false); return fmaxf(asf(s[0]), asf(s[1])); }
DI float tr32(float a, float b) { auto s = __builtin_amdgcn_permlane32_swap(asu(a), asu(b), false, false); return asf(s[0]) + asf(s[1]); }
DI float tr16(float a, float b) { auto s = __builtin_amdgcn_permlane16_swap(asu(a), asu(b), false, false); return asf(s[0]) + asf(s[1]); }
template <int CTRL> DI unsigned dppu(unsigned x) { return (unsigned)__builtin_amdgcn_mov_dpp((int)x, CTRL, 0xf, 0xf, true); }
DI unsigned xor4(const v4u& a) { return (a.x ^ a.y) ^ (a.z ^ a.w); }
DI unsigned add4(const v4u& a) { return (a.x + a.y) + (a.z + a.w); }
DI unsigned wave_xor(unsigned x) {
  x ^= dppu<XOR1>(x); x ^= dppu<XOR2>(x); x ^= dppu<HMIR>(x); x ^= dppu<MIR>(x);
  auto s = __builtin_amdgcn_permlane16_swap(x, x, false, false); x = s[0] ^ s[1];
  auto t = __builtin_amdgcn_permlane32_swap(x, x, false, false); return t[0] ^ t[1];
}
DI unsigned wave_add(unsigned x) {
  x += dppu<XOR1>(x); x += dppu<XOR2>(x); x += dppu<HMIR>(x); x += dppu<MIR>(x);
  auto s = __builtin_amdgcn_permlane16_swap(x, x, false, false); x = s[0] + s[1];
  auto t = __builtin_amdgcn_permlane32_swap(x, x, false, false); return t[0] + t[1];
}
template <bool ADD> DI unsigned tred8(const unsigned (&v)[8], int lane) {
  const bool b2 = lane & 4, b1 = lane & 2, b0 = lane & 1;
  unsigned a[4], b[2];
#pragma unroll
  for (int k = 0; k < 4; ++k) { const unsigned keep = b2 ? v[k + 4] : v[k], send = b2 ? v[k] : v[k + 4], r = dppu<HMIR>(send); a[k] = ADD ? keep + r : keep ^ r; }
#pragma unroll
  for (int k = 0; k < 2; ++k) { const unsigned keep = b1 ? a[k + 2] : a[k], send = b1 ? a[k] : a[k + 2], r = dppu<XOR2>(send); b[k] = ADD ? keep + r : keep ^ r; }
  const unsigned keep = b0 ? b[1] : b[0], send = b0 ? b[0] : b[1], r = dppu<XOR1>(send);
  return ADD ? keep + r : keep ^ r;
}
DI float wave_sum(float x) { return xsum32(xsum16(sum_row16(x))); }

template <int N> DI void waitv() { asm volatile("s_waitcnt vmcnt(%0)" ::"i"(N) : "memory"); }
DI void waitl() { asm volatile("s_waitcnt lgkmcnt(0)" ::: "memory"); }
DI void cfence() { asm volatile("" ::: "memory"); }
DI void bar() { asm volatile("s_waitcnt lgkmcnt(0)\n\ts_barrier" ::: "memory"); }
#define TIE(x) asm volatile("" : "+v"(x))

DI void gload4(v4u& d, const void* sb, unsigned vo) { asm volatile("global_load_dwordx4 %0, %1, %2" : "=v"(d) : "v"(vo), "s"(sb) : "memory"); }
DI void gload4nt(v4u& d, const void* sb, unsigned vo) { asm volatile("global_load_dwordx4 %0, %1, %2 nt" : "=v"(d) : "v"(vo), "s"(sb) : "memory"); }
DI void gloadh(unsigned& d, const void* sb, unsigned vo) { asm volatile("global_load_ushort %0, %1, %2" : "=v"(d) : "v"(vo), "s"(sb) : "memory"); }
DI void gload2sc(u64& d, const void* sb, unsigned vo) { asm volatile("global_load_dwordx2 %0, %1, %2 sc1" : "=v"(d) : "v"(vo), "s"(sb) : "memory"); }
DI void gstore4sc(const v4u& d, void* sb, unsigned vo) { asm volatile("global_store_dwordx4 %0, %1, %2 sc1\n\ts_nop 1" ::"v"(vo), "v"(d), "s"(sb) : "memory"); }
DI void dma16(const void* sb, unsigned vo, unsigned ldsaddr) {
  unsigned keep;
  asm volatile("s_mov_b32 %0, m0\n\ts_mov_b32 m0, %3\n\ts_nop 0\n\tglobal_load_lds_dwordx4 %1, %2 nt\n\ts_mov_b32 m0, %0"
               : "=&s"(keep) : "v"(vo), "s"(sb), "s"(ldsaddr) : "memory");
}


DI void sig(LAS unsigned* prog, int idx, unsigned v, int lane) {
  waitl();
  if (lane == 0) *(volatile LAS unsigned*)(prog + idx) = v;
  cfence();
}
DI void wait4(LAS unsigned* prog, int first, unsigned v) {
  for (;;) {
    v4u a = *(volatile LAS v4u*)(prog + first);
    unsigned mn = min(min(a.x, a.y), min(a.z, a.w));
    if (__builtin_amdgcn_readfirstlane(mn) >= v) break;
  }
  cfence();
}
DI void wait1(LAS unsigned* prog, int idx, unsigned v) {
  for (;;) {
    unsigned a = *(volatile LAS unsigned*)(prog + idx);
    if (__builtin_amdgcn_readfirstlane(a) >= v) break;
  }
  cfence();
}
DI unsigned max4(const v4u& a) { return max(max(a.x, a.y), max(a.z, a.w)); }

DI void fma8(float (&a0)[8], float (&a1)[8], const v4u& w, float x0, float x1) {
#pragma unroll
  for (int k = 0; k < 4; ++k) {
    const float lo = blo(w[k]), hi = bhi(w[k]);
    a0[2 * k] = fmaf(lo, x0, a0[2 * k]); a1[2 * k] = fmaf(lo, x1, a1[2 * k]);
    a0[2 * k + 1] = fmaf(hi, x0, a0[2 * k + 1]); a1[2 * k + 1] = fmaf(hi, x1, a1[2 * k + 1]);
  }
}

constexpr int OOB = 0x70000000;
template <int N> DI void poll_recs(v4u (&pv)[N], __amdgpu_buffer_rsrc_t rs, int base, bool hiHalf = false) {
  for (unsigned spin = 0; spin < SPIN_CAP; ++spin) {
#pragma unroll
    for (int i = 0; i < N; ++i) pv[i] = __builtin_amdgcn_raw_buffer_load_b128(rs, (N == 14 && i == 13 && hiHalf) ? OOB : base + i * 1024, 0, AUX_SC1);
    cfence();
    unsigned m = max4(pv[0]);
#pragma unroll
    for (int i = 1; i < N; ++i) m = max(m, max4(pv[i]));
    if (__all(m != SENT)) break;
    __builtin_amdgcn_s_sleep(SLP);
  }
}
template <int N> DI f32x4 own_reduce(__amdgpu_buffer_rsrc_t rs, int base, int lane) {
  v4u pv[N];
  poll_recs<N>(pv, rs, base + lane * 16, lane >= 32);
  f32x4 s = __builtin_bit_cast(f32x4, pv[0]);
#pragma unroll
  for (int i = 1; i < N; ++i) s += __builtin_bit_cast(f32x4, pv[i]);
#pragma unroll
  for (int k = 0; k < 4; ++k) { float t = s[k]; t += dpp<ROR8>(t); t += dpp<ROR4>(t); s[k] = xsum32(xsum16(t)); }
  return s;
}
DI f32x4 gather8(__amdgpu_buffer_rsrc_t rs, int base, int lane) {
  v4u pv[8];
  poll_recs<8>(pv, rs, base + lane * 16);
  f32x4 s = __builtin_bit_cast(f32x4, pv[0]);
#pragma unroll
  for (int i = 1; i < 8; ++i) s += __builtin_bit_cast(f32x4, pv[i]);
  return s;
}
DI void publish4(__amdgpu_buffer_rsrc_t rs, int off, const f32x4& s) {
  const v4u o = {sanef(s[0]), sanef(s[1]), sanef(s[2]), sanef(s[3])};
  __builtin_amdgcn_raw_buffer_store_b128(o, rs, off, 0, AUX_SC1);
}
DI void split4(const f32x4& v, v2u& hi, v2u& lo) {
  hi.x = cvtpk(v[0], v[1]); hi.y = cvtpk(v[2], v[3]);
  lo.x = cvtpk(v[0] - blo(hi.x), v[1] - bhi(hi.x)); lo.y = cvtpk(v[2] - blo(hi.y), v[3] - bhi(hi.y));
}
DI bf16x8 wfrag(LAS unsigned char* p, int pitch) {
  const s16x4 a = __builtin_amdgcn_ds_read_tr16_b64_v4i16((LAS s16x4*)p);
  const s16x4 b = __builtin_amdgcn_ds_read_tr16_b64_v4i16((LAS s16x4*)(p + 4 * pitch));
  return __builtin_shufflevector(a, b, 0, 1, 2, 3, 4, 5, 6, 7);
}
DI f32x4 mfma16(const bf16x8& a, const v4u& b, const f32x4& c) {
  return __builtin_amdgcn_mfma_f32_16x16x32_bf16(a, __builtin_bit_cast(bf16x8, b), c, 0, 0, 0);
}
DI f32x4 hilo(const f32x4& d) { f32x4 r; _Pragma("unroll") for (int k = 0; k < 4; ++k) r[k] = d[k] + dpp<XOR2>(d[k]); return r; }

__global__ void __launch_bounds__(NT, 3)
fused(const u16* __restrict__ x, const u16* __restrict__ ckv, const u16* __restrict__ wq, const u16* __restrict__ wuk,
      const u16* __restrict__ wuv, const u16* __restrict__ wo, const u16* __restrict__ wgt, const u16* __restrict__ wup,
      const u16* __restrict__ wdn, u16* __restrict__ out, unsigned char* __restrict__ ws) {
  extern __shared__ __attribute__((aligned(16))) unsigned char smem[];
  LAS unsigned char* L = (LAS unsigned char*)smem;
  LAS unsigned* prog = (LAS unsigned*)(L + L_CTRL);
  const int tid = threadIdx.x, lane = tid & 63, wv = __builtin_amdgcn_readfirstlane(tid >> 6);
  const int wg = blockIdx.x, xcd = wg & 7, jj = wg >> 3;
  const int h = xcd * 4 + (jj >> 3), cs = jj & 7;
  const int ab = wg >> 7, asplit = wg & 127;
  const bool ffn = jj < 27;
  const int cb = xcd * 9 + min(jj / 3, 8), kt = jj % 3, fi = cb * 3 + kt;
  const int nd = kt < 2 ? 24 : 16;
  const bool owner = jj >= 27 && (jj - 27) * 8 + xcd < 36;
  const int own = ((jj - 27) * 8 + xcd) * 2 + (wv & 1);
  const int rg = lane >> 4, ch = lane & 15;
  const __amdgpu_buffer_rsrc_t rs = __builtin_amdgcn_make_buffer_rsrc((void*)ws, 0, WS_BYTES, 0x00020000);
  const bool isC = wv < 4, isAdm = wv == 4, isDma = wv >= 8;
  if (wv < 4) __builtin_amdgcn_s_setprio(3);
  if (tid < 32) prog[tid] = 0u;
  bar();

  u64 ep = 0;
  float xmean0 = 0.f, xmean1 = 0.f;
  v4u xa, xb, xc, wqv[5], wukv[4]; unsigned xo0, xo1;
  if (isC) {
    const int m0 = 72 * cs + 18 * wv;
    gload2sc(ep, ws, WS_EPOCH);
    gload4(xa, x, lane * 16);
    gload4(xb, x, 1024 + lane * 16);
    gload4(xc, x, 2048 + (lane & 15) * 16);
    gloadh(xo0, x, (m0 + min(lane, 17)) * 2);
    gloadh(xo1, x, 1152 + (m0 + min(lane, 17)) * 2);
    const unsigned char* b = (const unsigned char*)wq + (size_t)m0 * 8192 + h * 256;
#pragma unroll
    for (int i = 0; i < 4; ++i) gload4nt(wqv[i], b + i * 4 * 8192, rg * 8192 + ch * 16);
    gload4nt(wqv[4], b, (16 + (rg & 1)) * 8192 + ch * 16);
    const unsigned char* b2 = (const unsigned char*)wuk + (size_t)(64 * cs + 16 * wv) * 8192 + h * 256;
#pragma unroll
    for (int i = 0; i < 4; ++i) gload4nt(wukv[i], b2 + i * 8192, (4 * rg) * 8192 + ch * 16);
  }
  if (isC) {
    waitv<9>();
    TIE(xa); TIE(xb); TIE(xc); TIE(xo0); TIE(xo1);
    float sa = 0.f, sb = 0.f, sc = 0.f;
#pragma unroll
    for (int k = 0; k < 4; ++k) { sa += blo(xa[k]) + bhi(xa[k]); sb += blo(xb[k]) + bhi(xb[k]); sc += blo(xc[k]) + bhi(xc[k]); }
    const bool b0 = lane < 8, c1 = lane < 16;
    const float m0 = wave_sum(sa + (b0 ? sb : 0.f)) * (1.0f / DM);
    const float m1 = wave_sum((b0 ? 0.f : sb) + (c1 ? sc : 0.f)) * (1.0f / DM);
    const float mb = b0 ? m0 : m1;
    xmean0 = m0; xmean1 = m1;
    float va = 0.f, vb = 0.f, vc = 0.f;
#pragma unroll
    for (int k = 0; k < 4; ++k) {
      float d;
      d = blo(xa[k]) - m0; va += d * d; d = bhi(xa[k]) - m0; va += d * d;
      d = blo(xb[k]) - mb; vb += d * d; d = bhi(xb[k]) - mb; vb += d * d;
      d = blo(xc[k]) - m1; vc += d * d; d = bhi(xc[k]) - m1; vc += d * d;
    }
    const float v0 = wave_sum(va + (b0 ? vb : 0.f)) * (1.0f / DM);
    const float v1 = wave_sum((b0 ? 0.f : vb) + (c1 ? vc : 0.f)) * (1.0f / DM);
    const float r0 = __builtin_amdgcn_rsqf(v0 + 1e-5f), r1 = __builtin_amdgcn_rsqf(v1 + 1e-5f);
    if (lane < 20) *(LAS f32x2*)(L + L_XL + (wv * 20 + lane) * 8) = lane < 18 ? f32x2{(blo(xo0) - m0) * r0, (blo(xo1) - m1) * r1} : f32x2{0.f, 0.f};
    waitl();
    f32x2 xr[5];
#pragma unroll
    for (int i = 0; i < 5; ++i) xr[i] = *(LAS f32x2*)(L + L_XL + (wv * 20 + 4 * i + rg) * 8);
    float a0[8], a1[8];
#pragma unroll
    for (int k = 0; k < 8; ++k) { a0[k] = 0.f; a1[k] = 0.f; }
    waitv<4>();
#pragma unroll
    for (int i = 0; i < 5; ++i) { TIE(wqv[i]); fma8(a0, a1, wqv[i], xr[i][0], xr[i][1]); }
    float t[8];
#pragma unroll
    for (int k = 0; k < 8; ++k) t[k] = tr32(a0[k], a1[k]);
    f32x4 o;
#pragma unroll
    for (int k = 0; k < 4; ++k) o[k] = tr16(t[k], t[k + 4]);
    *(LAS f32x4*)(L + L_QP + (wv * 256 + (lane >> 5) * 128 + ch * 8 + (rg & 1) * 4) * 4) = o;
    sig(prog, wv, 1u, lane);
  }

  if (!isC) {
    if (isAdm) {
      const v4u sv = {SENT, SENT, SENT, SENT};
#pragma unroll
      for (int i = 0; i < 7; ++i) gstore4sc(sv, ws, WS_CLR + wg * CLR_PER_WG + min(i * 1024 + lane * 16, CLR_PER_WG - 16));
      waitv<0>();
      sig(prog, 12, 1u, lane);
    }
    if (isDma) {
      __builtin_amdgcn_s_sleep(8);
      const unsigned char* b = (const unsigned char*)ckv + ((size_t)ab * SK + asplit * 128) * 1024;
      const unsigned lb = (unsigned)(uintptr_t)(L + L_C);
      bool deep = false;
#pragma unroll
      for (int i = 0; i < 32; ++i) {
        const int r = 4 * i + (wv - 8);
        const int sw = ((r & 3) << 2) | ((r >> 2) & 3);
        if (i >= DMA_D0) {
          if (!deep) deep = __builtin_amdgcn_readfirstlane(*(volatile LAS unsigned*)(prog + 14)) >= 1u;
          if (!deep) waitv<DMA_D0 - 1>(); else if (i >= DMA_DEPTH) waitv<DMA_DEPTH - 1>();
        }
        if (i == H1_TRIG) sig(prog, wv, 1u, lane);
        dma16(b + r * 1024, (unsigned)(((lane & 48) | ((lane & 15) ^ sw)) * 16), lb + r * 1024);
      }
      waitv<0>();
      sig(prog, wv, 2u, lane);
    }
    for (;;) {
      v4u a = *(volatile LAS v4u*)(prog);
      if (__builtin_amdgcn_readfirstlane(min(min(a.x, a.y), min(a.z, a.w))) >= 4u) break;
      __builtin_amdgcn_s_sleep(2);
    }
    cfence();
    v4u wuvr[2], wor[3], wdr[4], wgur[6];
    const int td = tid - 256, wd = wv - 4;
    const int orp = min(lane / 9, 6), oc9 = lane % 9;
    const int gr = (wd >> 1) * 48 + (lane >> 3);
    const int dg = min(td / 72, 6), oc = td % 72;
    {
      const unsigned char* b = (const unsigned char*)wuv + (size_t)(64 * cs + 8 * wd) * 8192 + h * 256;
      gload4nt(wuvr[0], b, rg * 8192 + ch * 16);
      gload4nt(wuvr[1], b + 4 * 8192, rg * 8192 + ch * 16);
      const unsigned char* wob = (const unsigned char*)wo + (size_t)(h * 128 + 16 * wd) * 1152 + cs * 144;
      gload4nt(wor[0], wob, orp * 1152 + oc9 * 16);
      gload4nt(wor[1], wob, (7 + orp) * 1152 + oc9 * 16);
      gload4nt(wor[2], wob, min(14 + orp, 15) * 1152 + oc9 * 16);
    }
    const unsigned char* gup = (const unsigned char*)((wd & 1) ? wup : wgt) + (size_t)(192 * kt) * 9216 + cb * 128;
    if (ffn) {
#pragma unroll
      for (int i = 0; i < NB1; ++i) gload4nt(wgur[i], gup + (size_t)i * 8 * 9216, gr * 9216 + (lane & 7) * 16);
      waitv<NB1>();
    } else {
      waitv<0>();
    }
    for (;;) {
      v4u a = *(volatile LAS v4u*)(prog);
      if (__builtin_amdgcn_readfirstlane(min(min(a.x, a.y), min(a.z, a.w))) >= 7u) break;
      __builtin_amdgcn_s_sleep(2);
    }
    cfence();
    TIE(wuvr[0]); TIE(wuvr[1]); TIE(wor[0]); TIE(wor[1]); TIE(wor[2]);
    *(LAS v4u*)(L + I_UV + (8 * wd + rg) * 256 + ch * 16) = wuvr[0];
    *(LAS v4u*)(L + I_UV + (8 * wd + 4 + rg) * 256 + ch * 16) = wuvr[1];
    if (lane < 63) {
      *(LAS v4u*)(L + I_WO + (16 * wd + orp) * 144 + oc9 * 16) = wor[0];
      *(LAS v4u*)(L + I_WO + (16 * wd + 7 + orp) * 144 + oc9 * 16) = wor[1];
      if (orp < 2) *(LAS v4u*)(L + I_WO + (16 * wd + 14 + orp) * 144 + oc9 * 16) = wor[2];
    }
    sig(prog, wv, 3u, lane);
    if (ffn) {
      for (;;) {
        unsigned a = *(volatile LAS unsigned*)(prog + 13);
        if (__builtin_amdgcn_readfirstlane(a) >= 1u) break;
        __builtin_amdgcn_s_sleep(2);
      }
      cfence();
#pragma unroll
      for (int i = NB1; i < NB2; ++i) gload4nt(wgur[i], gup + (size_t)i * 8 * 9216, gr * 9216 + (lane & 7) * 16);
      for (;;) {
        unsigned a = *(volatile LAS unsigned*)(prog + 13);
        if (__builtin_amdgcn_readfirstlane(a) >= 2u) break;
        __builtin_amdgcn_s_sleep(2);
      }
      cfence();
#pragma unroll
      for (int i = NB2; i < 6; ++i) gload4nt(wgur[i], gup + (size_t)i * 8 * 9216, gr * 9216 + (lane & 7) * 16);
      const unsigned char* b3 = (const unsigned char*)wdn + (size_t)(cb * 64 + kt * 24) * 1152;
#pragma unroll
      for (int i = 0; i < 4; ++i) gload4nt(wdr[i], b3, min(dg + 7 * i, nd - 1) * 1152 + oc * 16);
      waitv<4>();
#pragma unroll
      for (int i = 0; i < 6; ++i) { TIE(wgur[i]); *(LAS v4u*)(L + I_GU + (gr + 8 * i) * 272 + (wd & 1) * 128 + (lane & 7) * 16) = wgur[i]; }
      sig(prog, wv, 4u, lane);
      waitv<0>();
      if (td < 504) {
#pragma unroll
        for (int i = 0; i < 4; ++i) { TIE(wdr[i]); *(LAS v4u*)(L + I_DN + (dg + 7 * i) * 1168 + oc * 16) = dg + 7 * i < nd ? wdr[i] : v4u{0u, 0u, 0u, 0u}; }
        if (dg < 4) *(LAS v4u*)(L + I_DN + (28 + dg) * 1168 + oc * 16) = v4u{0u, 0u, 0u, 0u};
      }
      sig(prog, wv, 5u, lane);
    }
    return;
  }

  TIE(ep);
  const unsigned elo = __builtin_amdgcn_readfirstlane((unsigned)ep), ehi = __builtin_amdgcn_readfirstlane((unsigned)(ep >> 32));
  const unsigned tag = elo ^ 0x5bd1e995u, saltA = ehi + elo * 0x85EBCA6Bu, saltB = (elo ^ ehi) * 0xC2B2AE35u + 0x27D4EB2Fu;
  if (wv == 0) {
    wait4(prog, 0, 1u);
    f32x4 s = *(LAS f32x4*)(L + L_QP + lane * 16);
#pragma unroll
    for (int w = 1; w < 4; ++w) s += *(LAS f32x4*)(L + L_QP + w * 1024 + lane * 16);
    {
      const v4u su = __builtin_bit_cast(v4u, s);
      __builtin_amdgcn_raw_buffer_store_b128(su, rs, WS_Q0 + (h * 8 + cs) * 1024 + lane * 16, 0, AUX_SC1);
      const v4u tr = {tag, wave_xor(xor4(su)) ^ saltA, wave_add(add4(su)) + saltB, tag};
      __builtin_amdgcn_raw_buffer_store_b128(tr, rs, lane == 0 ? WS_T0 + (h * 8 + cs) * 128 : OOB, 0, AUX_SC1);
    }
    v4u pv[8];
    __builtin_amdgcn_s_sleep(PRE_0);
    for (unsigned spin = 0; spin < SPIN_CAP; ++spin) {
      const v4u tr = __builtin_amdgcn_raw_buffer_load_b128(rs, WS_T0 + (h * 8 + (lane & 7)) * 128, 0, AUX_SC1);
#pragma unroll
      for (int i = 0; i < 8; ++i) pv[i] = __builtin_amdgcn_raw_buffer_load_b128(rs, WS_Q0 + (h * 8 + i) * 1024 + lane * 16, 0, AUX_SC1);
      cfence();
      if (__all(tr.x == tag && tr.w == tag)) {
        unsigned vx[8], va[8];
#pragma unroll
        for (int i = 0; i < 8; ++i) { vx[i] = xor4(pv[i]); va[i] = add4(pv[i]); }
        unsigned cx = tred8<false>(vx, lane), ca = tred8<true>(va, lane);
        cx ^= dppu<ROR8>(cx); ca += dppu<ROR8>(ca);
        { auto t = __builtin_amdgcn_permlane16_swap(cx, cx, false, false); cx = t[0] ^ t[1]; }
        { auto t = __builtin_amdgcn_permlane16_swap(ca, ca, false, false); ca = t[0] + t[1]; }
        { auto t = __builtin_amdgcn_permlane32_swap(cx, cx, false, false); cx = t[0] ^ t[1]; }
        { auto t = __builtin_amdgcn_permlane32_swap(ca, ca, false, false); ca = t[0] + t[1]; }
        if (__all((cx ^ saltA) == tr.y && ca + saltB == tr.z)) break;
      }
      __builtin_amdgcn_s_sleep(SL0);
    }
    f32x4 qs = __builtin_bit_cast(f32x4, pv[0]);
#pragma unroll
    for (int i = 1; i < 8; ++i) qs += __builtin_bit_cast(f32x4, pv[i]);
    *(LAS f32x4*)(L + L_QV + lane * 16) = qs;
    sig(prog, 14, 1u, lane);
  } else {
    wait1(prog, 14, 1u);
  }
  {
    LAS unsigned char* qv = L + L_QV;
    const f32x4 q00 = *(LAS f32x4*)(qv + (ch * 8) * 4), q01 = *(LAS f32x4*)(qv + (ch * 8 + 4) * 4);
    const f32x4 q10 = *(LAS f32x4*)(qv + (128 + ch * 8) * 4), q11 = *(LAS f32x4*)(qv + (128 + ch * 8 + 4) * 4);
    float d0[4], d1[4];
    waitv<0>();
#pragma unroll
    for (int i = 0; i < 4; ++i) {
      TIE(wukv[i]);
      const v4u w = wukv[i];
      float s0 = blo(w[0]) * q00[0], s1 = blo(w[0]) * q10[0];
      s0 = fmaf(bhi(w[0]), q00[1], s0); s1 = fmaf(bhi(w[0]), q10[1], s1);
      s0 = fmaf(blo(w[1]), q00[2], s0); s1 = fmaf(blo(w[1]), q10[2], s1);
      s0 = fmaf(bhi(w[1]), q00[3], s0); s1 = fmaf(bhi(w[1]), q10[3], s1);
      s0 = fmaf(blo(w[2]), q01[0], s0); s1 = fmaf(blo(w[2]), q11[0], s1);
      s0 = fmaf(bhi(w[2]), q01[1], s0); s1 = fmaf(bhi(w[2]), q11[1], s1);
      s0 = fmaf(blo(w[3]), q01[2], s0); s1 = fmaf(blo(w[3]), q11[2], s1);
      s0 = fmaf(bhi(w[3]), q01[3], s0); s1 = fmaf(bhi(w[3]), q11[3], s1);
      d0[i] = sum_row16(s0); d1[i] = sum_row16(s1);
    }
    constexpr float QS = 0.08838834764831845f * 1.4426950408889634f;
    const bool sel = ch & 1;
    v2u o;
    o.x = cvtpk((sel ? d1[0] : d0[0]) * QS, (sel ? d1[1] : d0[1]) * QS);
    o.y = cvtpk((sel ? d1[2] : d0[2]) * QS, (sel ? d1[3] : d0[3]) * QS);
    if (ch < 2) *(LAS v2u*)(L + L_QO + (ch & 1) * 128 + (16 * wv + 4 * rg) * 2) = o;
  }
  sig(prog, wv, 3u, lane);
  if (wv == 0) {
    wait4(prog, 0, 3u);
    const v4u o = *(LAS v4u*)(L + L_QO + (lane & 15) * 16);
    __builtin_amdgcn_raw_buffer_store_b128(o, rs, lane < 16 ? WS_QA + (lane >> 3) * 32768 + h * 1024 + cs * 128 + (lane & 7) * 16 : OOB, 0, AUX_SC1);
    unsigned cx = xor4(o), ca = add4(o);
    cx ^= dppu<XOR1>(cx); cx ^= dppu<XOR2>(cx); cx ^= dppu<HMIR>(cx);
    ca += dppu<XOR1>(ca); ca += dppu<XOR2>(ca); ca += dppu<HMIR>(ca);
    const v4u tr = {tag, cx ^ saltA, ca + saltB, tag};
    __builtin_amdgcn_raw_buffer_store_b128(tr, rs, (lane & 7) < 2 ? WS_T1 + ((lane >> 4) + 4 * (lane & 1)) * 8192 + ((lane >> 3) & 1) * 4096 + (h * 8 + cs) * 16 : OOB, 0, AUX_SC1);
    sig(prog, 14, 2u, lane);
  } else {
    wait1(prog, 14, 2u);
  }
  wait4(prog, 8, 1u);
  {
    const int base = WS_QA + ab * 32768;
    v4u qf[8];
    for (unsigned spin = 0; spin < SPIN_CAP; ++spin) {
      const v4u tr = __builtin_amdgcn_raw_buffer_load_b128(rs, WS_T1 + (wg & (NREP - 1)) * 8192 + ab * 4096 + ((8 * wv + (lane & 7)) * 8 + (lane >> 3)) * 16, 0, AUX_SC1);
#pragma unroll
      for (int i = 0; i < 8; ++i) qf[i] = __builtin_amdgcn_raw_buffer_load_b128(rs, base + (8 * wv + i) * 1024 + lane * 16, 0, AUX_SC1);
      cfence();
      if (__all(tr.x == tag && tr.w == tag)) {
        unsigned vx[8], va[8];
#pragma unroll
        for (int i = 0; i < 8; ++i) { vx[i] = xor4(qf[i]); va[i] = add4(qf[i]); }
        const unsigned cx = tred8<false>(vx, lane), ca = tred8<true>(va, lane);
        if (__all((cx ^ saltA) == tr.y && ca + saltB == tr.z)) break;
      }
      __builtin_amdgcn_s_sleep(SL1);
    }
    if (lane < 62) {
#pragma unroll
      for (int i = 0; i < 8; ++i) *(LAS v4u*)(L + L_S + (8 * wv + i) * QPITCH + ((lane ^ (wv >> 1)) * 16)) = qf[i];
    }
  }
  sig(prog, wv, 4u, lane);
  wait4(prog, 0, 4u);
  const v4u q31 = __builtin_amdgcn_raw_buffer_load_b128(rs, WS_QA + ab * 32768 + (lane & 31) * 1024 + 992 + (lane >> 5) * 16, 0, AUX_SC1);
  wait4(prog, 8, 2u);
  const int r31 = lane & 31, hh = lane >> 5;
  f32x16 sacc;
#pragma unroll
  for (int i = 0; i < 16; ++i) sacc[i] = 0.f;
  {
    const int row = 32 * wv + r31;
    const int sw = ((row & 3) << 2) | ((row >> 2) & 3);
    LAS unsigned char* arow = L + L_C + row * 1024;
    LAS unsigned char* brow = L + L_S + r31 * QPITCH + ((hh ^ (r31 >> 4)) * 16);
    constexpr int W = 6;
    v4u af[32], bf[32];
#pragma unroll
    for (int s = 0; s < W; ++s) {
      const int c64 = 2 * s + hh;
      af[s] = *(LAS v4u*)(arow + (c64 >> 4) * 256 + (((c64 & 15) ^ sw) * 16));
      bf[s] = *(LAS v4u*)(brow + s * 32);
    }
    __builtin_amdgcn_sched_barrier(0);
#pragma unroll
    for (int s = 0; s < 32; ++s) {
      if (s + W < 32) {
        const int c64 = 2 * (s + W) + hh;
        af[s + W] = *(LAS v4u*)(arow + (c64 >> 4) * 256 + (((c64 & 15) ^ sw) * 16));
        bf[s + W] = s + W < 31 ? *(LAS v4u*)(brow + (s + W) * 32) : q31;
      }
      sacc = __builtin_amdgcn_mfma_f32_32x32x16_bf16(__builtin_bit_cast(bf16x8, af[s]), __builtin_bit_cast(bf16x8, bf[s]), sacc, 0, 0, 0);
      __builtin_amdgcn_sched_barrier(0);
    }
  }
  float mx = sacc[0];
#pragma unroll
  for (int i = 1; i < 16; ++i) mx = fmaxf(mx, sacc[i]);
  mx = xmax32(mx);
  if (lane < 32) *(LAS float*)(L + L_MX + (wv * 32 + lane) * 4) = mx;
  sig(prog, wv, 5u, lane);
  wait4(prog, 0, 5u);
#pragma unroll
  for (int w = 0; w < 4; ++w) mx = fmaxf(mx, *(LAS float*)(L + L_MX + (w * 32 + r31) * 4));
  {
    float ls = 0.f;
    v4u pf[2];
#pragma unroll
    for (int i = 0; i < 8; ++i) {
      const float p0 = __builtin_amdgcn_exp2f(sacc[2 * i] - mx), p1 = __builtin_amdgcn_exp2f(sacc[2 * i + 1] - mx);
      const unsigned pk = cvtpk(p0, p1);
      ls += blo(pk) + bhi(pk);
      pf[i >> 2][i & 3] = pk;
    }
    ls = xsum32(ls);
    *(LAS v4u*)(L + L_P + (wv * 2 + 0) * 1024 + lane * 16) = pf[0];
    *(LAS v4u*)(L + L_P + (wv * 2 + 1) * 1024 + lane * 16) = pf[1];
    if (lane < 32) *(LAS float*)(L + L_LS + (wv * 32 + lane) * 4) = ls;
  }
  sig(prog, wv, 6u, lane);
  wait4(prog, 0, 6u);
  float lt = 0.f;
  {
    f32x16 oacc[4];
#pragma unroll
    for (int cb = 0; cb < 4; ++cb)
#pragma unroll
      for (int i = 0; i < 16; ++i) oacc[cb][i] = 0.f;
    const int blk = (lane >> 4) & 1, q4 = (lane & 15) >> 2, p4 = lane & 3;
    LAS unsigned char* tb = L + L_C + (4 * hh + q4) * 1024 + wv * 256 + 8 * (p4 & 1);
    v4u pb[8];
#pragma unroll
    for (int ks = 0; ks < 8; ++ks) pb[ks] = *(LAS v4u*)(L + L_P + ks * 1024 + lane * 16);
#pragma unroll
    for (int w = 0; w < 4; ++w) lt += *(LAS float*)(L + L_LS + (w * 32 + r31) * 4);
    const float inv = __builtin_amdgcn_rcpf(lt);
    LAS unsigned char* st = L + L_ST + wv * 4096;
    LAS unsigned char* stw = st + r31 * 128 + hh * 8;
    LAS unsigned char* str_ = st + (lane >> 3) * 128 + (((lane & 7) ^ (lane >> 3)) * 16);
    const int pofs = WS_PART + ((((lane >> 3) * 8 + 2 * wv) * 2 + ab) * 128 + asplit) * 128 + (lane & 7) * 16;
    constexpr int W = 5;
    s16x4 a[32][2];
    v4u ov[4];
#define CBK(n) (2 * ((n) >> 4) + ((n) & 1))
#define KSK(n) (((n) >> 1) & 7)
#define TRLD(n) { _Pragma("unroll") for (int t = 0; t < 2; ++t) { \
      const int chunk = ((CBK(n) ^ q4) << 2) | ((2 * blk + (p4 >> 1)) ^ (2 * t + hh)); \
      a[n][t] = __builtin_amdgcn_ds_read_tr16_b64_v4i16((LAS s16x4*)(tb + (16 * KSK(n) + 8 * t) * 1024 + chunk * 16)); } }
#define STG(half, k) { const f32x16& o = oacc[2 * (half) + ((k) >> 2)]; const int g = (k) & 3; v2u pk; \
      pk.x = cvtpk(o[4 * g] * inv, o[4 * g + 1] * inv); pk.y = cvtpk(o[4 * g + 2] * inv, o[4 * g + 3] * inv); \
      *(LAS v2u*)(stw + (((k) ^ (r31 & 7)) * 16)) = pk; }
#pragma unroll
    for (int n = 0; n < W; ++n) TRLD(n)
    __builtin_amdgcn_sched_barrier(0);
#pragma unroll
    for (int n = 0; n < 32; ++n) {
      if (n + W < 32) TRLD(n + W)
      const bf16x8 A = __builtin_shufflevector(a[n][0], a[n][1], 0, 1, 2, 3, 4, 5, 6, 7);
      oacc[CBK(n)] = __builtin_amdgcn_mfma_f32_32x32x16_bf16(A, __builtin_bit_cast(bf16x8, pb[KSK(n)]), oacc[CBK(n)], 0, 0, 0);
      if (n >= 18 && n < 26) STG(0, n - 18)
      if (n == 27) {
        sig(prog, wv, 7u, lane);
#pragma unroll
        for (int i = 0; i < 4; ++i) ov[i] = *(LAS v4u*)(str_ + i * 1024);
      }
      if (n == 30) {
#pragma unroll
        for (int i = 0; i < 4; ++i) __builtin_amdgcn_raw_buffer_store_b128(ov[i], rs, pofs + i * (8 * 8 * 2 * 128 * 128), 0, AUX_SC1);
      }
      __builtin_amdgcn_sched_barrier(0);
    }
#pragma unroll
    for (int k = 0; k < 8; ++k) STG(1, k)
    waitl();
#pragma unroll
    for (int i = 0; i < 4; ++i) ov[i] = *(LAS v4u*)(str_ + i * 1024);
#pragma unroll
    for (int i = 0; i < 4; ++i) __builtin_amdgcn_raw_buffer_store_b128(ov[i], rs, pofs + i * (8 * 8 * 2 * 128 * 128) + 2 * 128 * 128, 0, AUX_SC1);
    cfence();
#undef TRLD
#undef STG
#undef CBK
#undef KSK
    sig(prog, wv, 8u, lane);
    const unsigned lu = asu(mx + __builtin_amdgcn_logf(lt));
    wait1(prog, 12, 1u);
    waitv<0>();
    __builtin_amdgcn_raw_buffer_store_b128(v4u{tag, lu, lu ^ saltA, lu + saltB}, rs, lane < 32 ? WS_LSE + (((lane * 4 + wv) * 2 + ab) * 128 + asplit) * 16 : OOB, 0, AUX_SC1);
  }
  const int tq = ch >> 2, tp = lane & 3;
  const int trow = 8 * rg + tq, tcol = (tp >> 1) * 16 + (tp & 1) * 8;
  const f32x4 z4 = {0.f, 0.f, 0.f, 0.f};
  wait4(prog, 4, 3u); wait4(prog, 8, 3u);
  bf16x8 auv[2][2], awo[4], awo4[4];
#pragma unroll
  for (int ks = 0; ks < 2; ++ks)
#pragma unroll
    for (int t = 0; t < 2; ++t) auv[ks][t] = wfrag(L + I_UV + (32 * ks + trow) * 256 + (2 * wv + t) * 32 + tcol, 256);
#pragma unroll
  for (int ks = 0; ks < 4; ++ks) {
    awo[ks] = wfrag(L + I_WO + (32 * ks + trow) * 144 + wv * 32 + tcol, 144);
    awo4[ks] = wfrag(L + I_WO + (32 * ks + trow) * 144 + 128 + tcol, 144);
  }
  {
    const int rbase = WS_PART + ((h * 8 + cs) * 2 + (wv >> 1)) * 128 * 128;
    const int lbase = WS_LSE + ((h * 4 + (cs >> 1)) * 2 + (wv >> 1)) * 2048 + lane * 16;
    v4u pv[8];
    const int pbase = rbase + 64 * (wv & 1) * 128 + lane * 16;
    if ((wv & 1) == 0) {
      unsigned l0, l1;
      for (unsigned spin = 0; spin < SPIN_CAP; ++spin) {
        const v4u r0 = __builtin_amdgcn_raw_buffer_load_b128(rs, lbase, 0, AUX_SC1);
        const v4u r1 = __builtin_amdgcn_raw_buffer_load_b128(rs, lbase + 1024, 0, AUX_SC1);
        cfence();
        l0 = r0.y; l1 = r1.y;
        const unsigned bad = (r0.x ^ tag) | (r0.z ^ l0 ^ saltA) | (r0.w - l0 - saltB) | (r1.x ^ tag) | (r1.z ^ l1 ^ saltA) | (r1.w - l1 - saltB);
        if (__all(bad == 0u)) break;
        __builtin_amdgcn_s_sleep(SL2);
      }
      sig(prog, 18 + (wv >> 1), 1u, lane);
#pragma unroll
      for (int i = 0; i < 8; ++i) pv[i] = __builtin_amdgcn_raw_buffer_load_b128(rs, pbase + i * 1024, 0, AUX_SC1);
      const float m = xmax32(xmax16(max_row16(fmaxf(asf(l0), asf(l1)))));
      const float e0 = __builtin_amdgcn_exp2f(asf(l0) - m), e1 = __builtin_amdgcn_exp2f(asf(l1) - m);
      const float it = __builtin_amdgcn_rcpf(wave_sum(e0 + e1));
      wait4(prog, 0, 8u);
      *(LAS float*)(L + T_WG + wv * 256 + lane * 4) = e0 * it;
      *(LAS float*)(L + T_WG + wv * 256 + 256 + lane * 4) = e1 * it;
      sig(prog, 18 + (wv >> 1), 2u, lane);
    } else {
      wait1(prog, 18 + (wv >> 1), 1u);
#pragma unroll
      for (int i = 0; i < 8; ++i) pv[i] = __builtin_amdgcn_raw_buffer_load_b128(rs, pbase + i * 1024, 0, AUX_SC1);
      wait1(prog, 18 + (wv >> 1), 2u);
    }
    float acc[8];
#pragma unroll
    for (int k = 0; k < 8; ++k) acc[k] = 0.f;
#pragma unroll
    for (int i = 0; i < 8; ++i) {
      const float w = *(LAS float*)(L + T_WG + wv * 256 + (8 * i + (lane >> 3)) * 4);
#pragma unroll
      for (int k = 0; k < 4; ++k) { acc[2 * k] = fmaf(w, blo(pv[i][k]), acc[2 * k]); acc[2 * k + 1] = fmaf(w, bhi(pv[i][k]), acc[2 * k + 1]); }
    }
    float t[4], u2[2];
#pragma unroll
    for (int k = 0; k < 4; ++k) t[k] = tr32(acc[k], acc[k + 4]);
#pragma unroll
    for (int k = 0; k < 2; ++k) u2[k] = tr16(t[k], t[k + 2]);
    u2[0] += dpp<ROR8>(u2[0]); u2[1] += dpp<ROR8>(u2[1]);
    if ((lane & 8) == 0) *(LAS f32x2*)(L + T_CC + (wv * 64 + (lane & 7) * 8 + (lane >> 5) * 4 + ((lane >> 4) & 1) * 2) * 4) = f32x2{u2[0], u2[1]};
  }
  sig(prog, wv, 9u, lane);
  wait4(prog, 0, 9u);
  {
    f32x4 c[2][4];
#pragma unroll
    for (int ks = 0; ks < 2; ++ks) {
      LAS unsigned char* cp = L + T_CC + ((ch & 1) * 128 + 32 * ks + 8 * rg) * 4;
      c[ks][0] = *(LAS f32x4*)(cp); c[ks][1] = *(LAS f32x4*)(cp + 256); c[ks][2] = *(LAS f32x4*)(cp + 16); c[ks][3] = *(LAS f32x4*)(cp + 272);
    }
    f32x4 d[2] = {z4, z4};
#pragma unroll
    for (int ks = 0; ks < 2; ++ks) {
      v2u h0, l0, h1, l1;
      split4(c[ks][0] + c[ks][1], h0, l0); split4(c[ks][2] + c[ks][3], h1, l1);
      const v4u b = (ch & 2) ? v4u{l0.x, l0.y, l1.x, l1.y} : v4u{h0.x, h0.y, h1.x, h1.y};
#pragma unroll
      for (int t = 0; t < 2; ++t) d[t] = mfma16(auv[ks][t], b, d[t]);
    }
#pragma unroll
    for (int t = 0; t < 2; ++t) publish4(rs, ch < 2 ? WS_CX + (h * 8 + cs) * 1024 + (ch * 128 + 32 * wv + 16 * t + 4 * rg) * 4 : OOB, hilo(d[t]));
  }
  if (wv == 0) sig(prog, 13, 1u, lane);
  if (wv == 0) {
    __builtin_amdgcn_s_sleep(PRE_G);
    const f32x4 cx = gather8(rs, WS_CX + h * 8192, lane);
    v2u hi, lo;
    split4(cx, hi, lo);
    *(LAS v2u*)(L + T_CX + hh * 272 + r31 * 8) = hi;
    *(LAS v2u*)(L + T_CX + (2 + hh) * 272 + r31 * 8) = lo;
    sig(prog, 14, 3u, lane);
  } else {
    wait1(prog, 14, 3u);
  }
  {
    v4u b[4];
#pragma unroll
    for (int ks = 0; ks < 4; ++ks) b[ks] = *(LAS v4u*)(L + T_CX + (ch & 3) * 272 + (32 * ks + 8 * rg) * 2);
    f32x4 d = z4, d4 = z4;
#pragma unroll
    for (int ks = 0; ks < 4; ++ks) { d = mfma16(awo[ks], b[ks], d); d4 = mfma16(awo4[ks], b[ks], d4); }
    const int ob = WS_OP + (ch * 32 + h) * 2304 + (72 * cs + 4 * rg) * 4;
    publish4(rs, ch < 2 ? ob + 64 * wv : OOB, hilo(d));
    publish4(rs, (ch < 2 && rg < 2 && wv == 2) ? ob + 256 : OOB, hilo(d4));
  }
  if (wv == 0) sig(prog, 13, 2u, lane);
  if (owner) {
    if (wv < 2) {
      const v2u xv = *(const v2u*)(x + own * 16 + (lane & 3) * 4);
      __builtin_amdgcn_s_sleep(PRE_H4);
      f32x4 s;
      {
        v4u pv[2];
        const int ob = WS_OP + ((own >= 36 ? 32 : 0) + (lane >> 2)) * 2304 + (own % 36) * 64 + (lane & 3) * 16;
        for (unsigned spin = 0; spin < SPIN_CAP; ++spin) {
          pv[0] = __builtin_amdgcn_raw_buffer_load_b128(rs, ob, 0, AUX_SC1);
          pv[1] = __builtin_amdgcn_raw_buffer_load_b128(rs, ob + 16 * 2304, 0, AUX_SC1);
          cfence();
          if (__all(max(max4(pv[0]), max4(pv[1])) != SENT)) break;
          __builtin_amdgcn_s_sleep(SLP);
        }
        s = __builtin_bit_cast(f32x4, pv[0]) + __builtin_bit_cast(f32x4, pv[1]);
#pragma unroll
        for (int k = 0; k < 4; ++k) { float t = s[k]; t += dpp<ROR8>(t); t += dpp<ROR4>(t); s[k] = xsum32(xsum16(t)); }
      }
      const f32x4 hown = f32x4{blo(xv.x) + s[0], bhi(xv.x) + s[1], blo(xv.y) + s[2], bhi(xv.y) + s[3]};
      const f32x4 s2 = own_reduce<14>(rs, WS_DP + own * (NFF * 64), lane);
      v2u o;
      o.x = cvtpk(hown[0] + s2[0], hown[1] + s2[1]);
      o.y = cvtpk(hown[2] + s2[2], hown[3] + s2[3]);
      if (lane < 4) *(v2u*)(out + own * 16 + lane * 4) = o;
      if (own == 0 && lane == 0)
        __hip_atomic_store((u64*)(ws + WS_EPOCH), ep + 1ull, __ATOMIC_RELAXED, __HIP_MEMORY_SCOPE_AGENT);
    }
  } else if (ffn) {
    const int fb = wv >> 1;
    const v2u xv = *(const v2u*)(x + fb * 576 + 192 * kt + min(lane, 47) * 4);
    if (wv == 3) {
      if (lane < 24) *(LAS v4u*)(L + T_XN + 4 * 400 + lane * 16) = v4u{0x3F803F80u, 0x3F803F80u, 0x3F803F80u, 0x3F803F80u};
#pragma unroll
      for (int r = 5; r < 8; ++r) if (lane < 24) *(LAS v4u*)(L + T_XN + r * 400 + lane * 16) = v4u{0u, 0u, 0u, 0u};
    }
    f32x4 hs;
    {
      v4u pv[16];
      const int ob = lane < 48 ? WS_OP + (fb * 32 + 16 * (wv & 1)) * 2304 + 768 * kt + lane * 16 : OOB;
      __builtin_amdgcn_s_sleep(PRE_G);
      for (unsigned spin = 0; spin < SPIN_CAP; ++spin) {
#pragma unroll
        for (int i = 0; i < 16; ++i) pv[i] = __builtin_amdgcn_raw_buffer_load_b128(rs, ob + i * 2304, 0, AUX_SC1);
        cfence();
        unsigned m = max4(pv[0]);
#pragma unroll
        for (int i = 1; i < 16; ++i) m = max(m, max4(pv[i]));
        if (__all(m != SENT)) break;
        __builtin_amdgcn_s_sleep(SLH);
      }
      hs = __builtin_bit_cast(f32x4, pv[0]);
#pragma unroll
      for (int i = 1; i < 16; ++i) hs += __builtin_bit_cast(f32x4, pv[i]);
    }
    if (wv & 1) {
      if (lane < 48) *(LAS f32x4*)(L + T_HP + fb * 768 + lane * 16) = hs;
      sig(prog, 16 + fb, 1u, lane);
    } else {
      const float cm = fb ? xmean1 : xmean0;
      hs += f32x4{blo(xv.x) - cm, bhi(xv.x) - cm, blo(xv.y) - cm, bhi(xv.y) - cm};
      wait1(prog, 16 + fb, 1u);
      hs += *(LAS f32x4*)(L + T_HP + fb * 768 + min(lane, 47) * 16);
      if (lane >= 48) hs = z4;
      v2u hi, lo;
      split4(hs, hi, lo);
      if (lane < 48) {
        *(LAS v2u*)(L + T_XN + fb * 400 + lane * 8) = hi;
        *(LAS v2u*)(L + T_XN + (2 + fb) * 400 + lane * 8) = lo;
      }
      sig(prog, 16 + fb, 2u, lane);
      const float s1 = wave_sum((hs[0] + hs[1]) + (hs[2] + hs[3]));
      const float s2 = wave_sum((hs[0] * hs[0] + hs[1] * hs[1]) + (hs[2] * hs[2] + hs[3] * hs[3]));
      __builtin_amdgcn_raw_buffer_store_b64(v2u{sanef(s1), sanef(s2)}, rs, lane == 0 ? WS_GX + fi * GXREC + 1536 + fb * 8 : OOB, 0, AUX_SC1);
    }
    wait4(prog, 4, 4u); wait4(prog, 8, 4u);
    bf16x8 wa[9];
    {
      bf16x8 ag[6], au[6];
      LAS unsigned char* ap = L + I_GU + trow * 272 + wv * 32 + tcol;
#pragma unroll
      for (int ks = 0; ks < 6; ++ks) { ag[ks] = wfrag(ap + ks * 32 * 272, 272); au[ks] = wfrag(ap + ks * 32 * 272 + 128, 272); }
      wait1(prog, 16, 2u); wait1(prog, 17, 2u);
      f32x4 dg_ = z4, du_ = z4;
      LAS unsigned char* bp = L + T_XN + (ch & 7) * 400 + 8 * rg * 2;
      v4u b[6];
#pragma unroll
      for (int ks = 0; ks < 6; ++ks) b[ks] = *(LAS v4u*)(bp + ks * 64);
#pragma unroll
      for (int ks = 0; ks < 6; ++ks) { dg_ = mfma16(ag[ks], b[ks], dg_); du_ = mfma16(au[ks], b[ks], du_); }
      const int rb = WS_GX + fi * GXREC + (16 * wv + 4 * rg) * 4;
      publish4(rs, ch < 2 ? rb + ch * 256 : (ch == 4 ? rb + 1024 : OOB), hilo(dg_));
      publish4(rs, ch < 2 ? rb + 512 + ch * 256 : (ch == 4 ? rb + 1280 : OOB), hilo(du_));
      wait4(prog, 4, 5u); wait4(prog, 8, 5u);
#pragma unroll
      for (int t = 0; t < 9; ++t) wa[t] = wfrag(L + I_DN + trow * 1168 + (9 * wv + t) * 32 + tcol, 1168);
    }
    if (wv == 0) {
      v4u pv[3], pc[3]; v2u ps[3];
      const int gb = WS_GX + cb * 3 * GXREC;
      __builtin_amdgcn_s_sleep(PRE_G);
      for (unsigned spin = 0; spin < SPIN_CAP; ++spin) {
#pragma unroll
        for (int i = 0; i < 3; ++i) {
          pv[i] = __builtin_amdgcn_raw_buffer_load_b128(rs, gb + i * GXREC + lane * 16, 0, AUX_SC1);
          pc[i] = __builtin_amdgcn_raw_buffer_load_b128(rs, gb + i * GXREC + 1024 + hh * 256 + ch * 16, 0, AUX_SC1);
          ps[i] = __builtin_amdgcn_raw_buffer_load_b64(rs, gb + i * GXREC + 1536 + ((lane >> 4) & 1) * 8, 0, AUX_SC1);
        }
        cfence();
        unsigned m = 0;
#pragma unroll
        for (int i = 0; i < 3; ++i) m = max(max(m, max4(pv[i])), max(max4(pc[i]), max(ps[i].x, ps[i].y)));
        if (__all(m != SENT)) break;
        __builtin_amdgcn_s_sleep(SLP);
      }
      const float mu = (asf(ps[0].x) + asf(ps[1].x) + asf(ps[2].x)) * (1.0f / DM);
      const float var = fmaxf((asf(ps[0].y) + asf(ps[1].y) + asf(ps[2].y)) * (1.0f / DM) - mu * mu, 0.f);
      const float r = __builtin_amdgcn_rsqf(var + 1e-5f);
      const f32x4 csum = __builtin_bit_cast(f32x4, pc[0]) + __builtin_bit_cast(f32x4, pc[1]) + __builtin_bit_cast(f32x4, pc[2]);
      const f32x4 sgu = (__builtin_bit_cast(f32x4, pv[0]) + __builtin_bit_cast(f32x4, pv[1]) + __builtin_bit_cast(f32x4, pv[2]) - csum * mu) * r;
      f32x4 a;
#pragma unroll
      for (int k = 0; k < 4; ++k) {
        auto t = __builtin_amdgcn_permlane32_swap(asu(sgu[k]), asu(sgu[k]), false, false);
        const float g = asf(t[0]), u = asf(t[1]);
        a[k] = g * __builtin_amdgcn_rcpf(1.0f + __expf(-g)) * u;
      }
      v2u hi, lo;
      split4(a, hi, lo);
      *(LAS v2u*)(L + T_ACT + (2 * hh + ((lane >> 4) & 1)) * 176 + ch * 8) = hh ? lo : hi;
      if (lane < 16) *(LAS v2u*)(L + T_ACT + (lane >> 2) * 176 + 128 + (lane & 3) * 8) = v2u{0u, 0u};
      sig(prog, 14, 5u, lane);
    } else {
      wait1(prog, 14, 5u);
    }
    {
      const v4u b = *(LAS v4u*)(L + T_ACT + (ch & 3) * 176 + (24 * kt + 8 * rg) * 2);
      const int ob = ch < 2 ? WS_DP + ((ch * 36 + 9 * wv) * NFF + fi) * 64 + rg * 16 : OOB;
      f32x4 d[9];
#pragma unroll
      for (int t = 0; t < 9; ++t) d[t] = mfma16(wa[t], b, z4);
#pragma unroll
      for (int t = 0; t < 9; ++t) publish4(rs, ob + t * (NFF * 64), hilo(d[t]));
    }
  }
}
}

extern "C" void kernel_launch(void* const* d_in, const int* in_sizes, int n_in,
                              void* d_out, int out_size, void* d_ws, size_t ws_size,
                              hipStream_t stream) {
  using mk::u16;
  (void)hipFuncSetAttribute((const void*)mk::fused, hipFuncAttributeMaxDynamicSharedMemorySize, mk::L_BYTES);
  mk::fused<<<mk::NWG, mk::NT, mk::L_BYTES, stream>>>(
      (const u16*)d_in[0], (const u16*)d_in[1], (const u16*)d_in[2], (const u16*)d_in[3], (const u16*)d_in[4], (const u16*)d_in[5],
      (const u16*)d_in[7], (const u16*)d_in[8], (const u16*)d_in[9], (u16*)d_out, (unsigned char*)d_ws);
}
```
